```python
import torch

def _softmax(x, axis=-1):
    return torch.softmax(x, dim=axis)

def _gelu(x):
    return torch.nn.functional.gelu(x, approximate='tanh')

def _silu(x):
    return torch.nn.functional.silu(x)

def _sigmoid(x):
    return torch.sigmoid(x)

def _layernorm(x, axis=-1, eps=1e-05):
    m = x.mean(dim=axis, keepdim=True)
    v = x.var(dim=axis, keepdim=True, unbiased=False)
    return (x - m) / torch.sqrt(v + eps)

def _scatter_add(tgt, idx, src, axis=0):
    return tgt.clone().index_add_(axis, idx, src)

def _segment_sum(src, seg, n):
    out = torch.zeros((n,) + src.shape[1:], dtype=src.dtype)
    return out.index_add_(0, seg, src)

def _pad(x, pw):
    flat = tuple((v for pair in reversed(pw) for v in pair))
    return torch.nn.functional.pad(x, flat)
kernel_name = 'gated_mix__sliding_local_dilated__mixer_gmlp__prompt'

def setup_inputs():

    _w = lambda a: (lambda t: t.to(torch.bfloat16) if t.is_floating_point() else t)(torch.as_tensor(a))

    def _A_setup():
        B, S, D = (1, 2048, 512)
        return {'x': _w((lambda *s: torch.randn(*s, dtype=torch.bfloat16) * 0.05)(B, S, D)), 'wqkv': _w((lambda *s: torch.randn(*s, dtype=torch.bfloat16) * 0.8)(D, 3 * D)), 'wo': _w((lambda *s: torch.randn(*s, dtype=torch.bfloat16) * 0.05)(D, D))}
    _A_in = _A_setup()

    def _B_setup():
        B, S, D, F = (1, 2048, 512, 1376)
        Fh = F // 2
        return {'x': _w((lambda *s: torch.randn(*s, dtype=torch.bfloat16) * 0.05)(B, S, D)), 'w_in': _w((lambda *s: torch.randn(*s, dtype=torch.bfloat16) * 0.4)(D, 2 * Fh)), 'w_sp': _w(0.01 * (lambda *s: torch.randn(*s, dtype=torch.bfloat16) * 0.4)(S, S)), 'b_sp': _w(1.0 + 0.01 * (lambda *s: torch.randn(*s, dtype=torch.bfloat16) * 0.1)(S)), 'w_out': _w((lambda *s: torch.randn(*s, dtype=torch.bfloat16) * 0.1)(Fh, D))}
    _B_in = _B_setup()
    return {'A_x': _A_in['x'], 'A_wqkv': _A_in['wqkv'], 'A_wo': _A_in['wo'], 'B_x': _B_in['x'], 'B_w_in': _B_in['w_in'], 'B_w_sp': _B_in['w_sp'], 'B_b_sp': _B_in['b_sp'], 'B_w_out': _B_in['w_out'], 'gate_w': _w((lambda *s: torch.randn(*s, dtype=torch.bfloat16) * 0.05)(512) * 16.0)}

def reference(A_x, A_wqkv, A_wo, B_x, B_w_in, B_w_sp, B_b_sp, B_w_out, gate_w):

    def _A_ref(x, wqkv, wo):
        B, S, D = x.shape
        H = 32
        Dh = D // H
        W, dil = (8, 2)
        qkv = (lambda x, *d: x.permute(*d))((x @ wqkv).reshape(B, S, 3, H, Dh), 2, 0, 3, 1, 4)
        q, k, v = (qkv[0], qkv[1], qkv[2])
        scale = Dh ** (-0.5)
        pos = torch.arange(S)
        off = torch.arange(W) * dil
        idx = pos[:, None] - off[None, :]
        mask = idx >= 0
        idx_c = torch.where(mask, idx, 0).reshape(-1)
        kg = k[:, :, idx_c, :].reshape(B, H, S, W, Dh)
        vg = v[:, :, idx_c, :].reshape(B, H, S, W, Dh)
        logits = torch.einsum('bhsd,bhswd->bhsw', q, kg) * scale
        logits = torch.where(mask[None, None, :, :], logits, -1000000000.0)
        attn = _softmax(logits, axis=-1)
        o = torch.einsum('bhsw,bhswd->bhsd', attn, vg)
        return (lambda x, *d: x.permute(*d))(o, 0, 2, 1, 3).reshape(B, S, D) @ wo

    def _B_ref(x, w_in, w_sp, b_sp, w_out):
        B, S, D = x.shape
        F = w_in.shape[-1]
        Fh = F // 2
        z = _gelu(torch.einsum('bsd,df->bsf', x, w_in))
        z1 = z[..., :Fh].clone()
        z2 = _layernorm(z[..., Fh:], axis=-1)
        g = torch.einsum('ts,bsf->btf', w_sp, z2) + b_sp[None, :, None]
        return torch.einsum('bsf,fd->bsd', z1 * g, w_out)
    x = A_x
    g = _sigmoid((x * gate_w).sum(-1, keepdims=True))
    a = _A_ref(x, A_wqkv, A_wo)
    b = _B_ref(x, B_w_in, B_w_sp, B_b_sp, B_w_out)
    return g * a + (1 - g) * b
```

```cpp
#include <hip/hip_runtime.h>
#include <hip/hip_bf16.h>

typedef __hip_bfloat16 bf16;
static __device__ __forceinline__ float b2f(bf16 v){ return __bfloat162float(v); }

__global__ void gemm_naive(const bf16* A, int lda, const bf16* B, int ldb, float* C, int ldc, int M, int N, int K){
  __shared__ float sA[16][17], sB[16][17];
  int tx=threadIdx.x, ty=threadIdx.y;
  int row=blockIdx.y*16+ty, col=blockIdx.x*16+tx;
  float acc=0.f;
  for(int k0=0;k0<K;k0+=16){
    sA[ty][tx] = (row<M && k0+tx<K)? b2f(A[(long)row*lda+k0+tx]) : 0.f;
    sB[ty][tx] = (k0+ty<K && col<N)? b2f(B[(long)(k0+ty)*ldb+col]) : 0.f;
    __syncthreads();
    for(int k=0;k<16;++k) acc += sA[ty][k]*sB[k][tx];
    __syncthreads();
  }
  if(row<M && col<N) C[(long)row*ldc+col]=acc;
}

__global__ void attn_naive(const float* qkv, bf16* o){
  int idx=blockIdx.x*blockDim.x+threadIdx.x;
  if(idx>=2048*32) return;
  int t=idx>>5, h=idx&31;
  const float* q=qkv+(long)t*1536+h*16;
  float l[8]; float m=-1e30f;
  for(int w=0;w<8;++w){
    int s=t-2*w;
    if(s>=0){
      const float* k=qkv+(long)s*1536+512+h*16;
      float d=0.f; for(int i=0;i<16;++i) d+=q[i]*k[i];
      l[w]=d*0.25f;
    } else l[w]=-1e9f;
    m=fmaxf(m,l[w]);
  }
  float sum=0.f;
  for(int w=0;w<8;++w){ l[w]=__expf(l[w]-m); sum+=l[w]; }
  float inv=1.f/sum;
  for(int i=0;i<16;++i){
    float a=0.f;
    for(int w=0;w<8;++w){ int s=t-2*w; if(s<0) s=0; a+=l[w]*inv*qkv[(long)s*1536+1024+h*16+i]; }
    o[(long)t*512+h*16+i]=__float2bfloat16(a);
  }
}

__device__ __forceinline__ float gelu_f(float x){
  float u=0.7978845608028654f*(x+0.044715f*x*x*x);
  return 0.5f*x*(1.f+tanhf(u));
}
__global__ void gelu_ln(const float* zp, float* z1, bf16* z2n){
  int s=blockIdx.x; __shared__ float red[256]; __shared__ float zz[688];
  int tid=threadIdx.x;
  float sum=0.f;
  for(int f=tid;f<688;f+=256){ z1[(long)s*688+f]=gelu_f(zp[(long)s*1376+f]); float v=gelu_f(zp[(long)s*1376+688+f]); zz[f]=v; sum+=v; }
  red[tid]=sum; __syncthreads();
  for(int o=128;o>0;o>>=1){ if(tid<o) red[tid]+=red[tid+o]; __syncthreads(); }
  float mean=red[0]/688.f; __syncthreads();
  float sq=0.f;
  for(int f=tid;f<688;f+=256){ float d=zz[f]-mean; sq+=d*d; }
  red[tid]=sq; __syncthreads();
  for(int o=128;o>0;o>>=1){ if(tid<o) red[tid]+=red[tid+o]; __syncthreads(); }
  float r=rsqrtf(red[0]/688.f+1e-5f);
  for(int f=tid;f<688;f+=256) z2n[(long)s*688+f]=__float2bfloat16((zz[f]-mean)*r);
}
__global__ void mul_u(const float* z1, const float* g2, const bf16* bsp, bf16* u){
  int idx=blockIdx.x*blockDim.x+threadIdx.x; if(idx>=2048*688) return;
  int t=idx/688;
  u[idx]=__float2bfloat16(z1[idx]*(g2[idx]+b2f(bsp[t])));
}
__global__ void final_k(const bf16* x, const bf16* gw, const float* a, const float* b, bf16* out){
  int t=blockIdx.x; int tid=threadIdx.x; __shared__ float red[256];
  float s=0.f; for(int d=tid;d<512;d+=256) s+=b2f(x[(long)t*512+d])*b2f(gw[d]);
  red[tid]=s; __syncthreads();
  for(int o=128;o>0;o>>=1){ if(tid<o) red[tid]+=red[tid+o]; __syncthreads(); }
  float g=1.f/(1.f+__expf(-red[0]));
  for(int d=tid;d<512;d+=256){ long i=(long)t*512+d; out[i]=__float2bfloat16(g*a[i]+(1.f-g)*b[i]); }
}

extern "C" void kernel_launch(void* const* d_in, const int* in_sizes, int n_in,
                              void* d_out, int out_size, void* d_ws, size_t ws_size,
                              hipStream_t stream) {
  const bf16* x=(const bf16*)d_in[0]; const bf16* wqkv=(const bf16*)d_in[1]; const bf16* wo=(const bf16*)d_in[2];
  const bf16* w_in=(const bf16*)d_in[4]; const bf16* w_sp=(const bf16*)d_in[5]; const bf16* b_sp=(const bf16*)d_in[6];
  const bf16* w_out=(const bf16*)d_in[7]; const bf16* gw=(const bf16*)d_in[8];
  char* ws=(char*)d_ws;
  float* qkv=(float*)ws; ws+=(size_t)2048*1536*4;
  float* zp=(float*)ws; ws+=(size_t)2048*1376*4;
  float* z1=(float*)ws; ws+=(size_t)2048*688*4;
  float* g2=(float*)ws; ws+=(size_t)2048*688*4;
  float* a=(float*)ws; ws+=(size_t)2048*512*4;
  float* b=(float*)ws; ws+=(size_t)2048*512*4;
  bf16* o=(bf16*)ws; ws+=(size_t)2048*512*2;
  bf16* z2n=(bf16*)ws; ws+=(size_t)2048*688*2;
  bf16* u=(bf16*)ws; ws+=(size_t)2048*688*2;
  dim3 blk(16,16);
  gemm_naive<<<dim3(1536/16,128),blk,0,stream>>>(x,512,wqkv,1536,qkv,1536,2048,1536,512);
  gemm_naive<<<dim3(1376/16,128),blk,0,stream>>>(x,512,w_in,1376,zp,1376,2048,1376,512);
  attn_naive<<<2048*32/256,256,0,stream>>>(qkv,o);
  gelu_ln<<<2048,256,0,stream>>>(zp,z1,z2n);
  gemm_naive<<<dim3(688/16,128),blk,0,stream>>>(w_sp,2048,z2n,688,g2,688,2048,688,2048);
  mul_u<<<(2048*688+255)/256,256,0,stream>>>(z1,g2,b_sp,u);
  gemm_naive<<<dim3(512/16,128),blk,0,stream>>>(o,512,wo,512,a,512,2048,512,512);
  gemm_naive<<<dim3(512/16,128),blk,0,stream>>>(u,688,w_out,512,b,512,2048,512,688);
  final_k<<<2048,256,0,stream>>>(x,gw,a,b,(bf16*)d_out);
}
```

```cpp
#include <hip/hip_runtime.h>
#include <hip/hip_bf16.h>
#include <stdint.h>

#define DI __device__ __forceinline__
#define GAS __attribute__((address_space(1)))
#define LAS __attribute__((address_space(3)))
typedef __attribute__((ext_vector_type(8))) __bf16 bf16x8;
typedef __attribute__((ext_vector_type(2))) __bf16 bf16x2;
typedef __attribute__((ext_vector_type(4))) short s16x4;
typedef __attribute__((ext_vector_type(8))) short s16x8;
typedef __attribute__((ext_vector_type(4))) float f32x4;
typedef __attribute__((ext_vector_type(2))) float f32x2;
typedef __attribute__((ext_vector_type(4))) unsigned u32x4;
typedef __attribute__((ext_vector_type(2))) unsigned u32x2;
typedef unsigned long long u64;
typedef __amdgpu_buffer_rsrc_t srd_t;

constexpr unsigned WS_GEN = 0, WS_FLAGS = 4096, FLAG_STRIDE = 128;
constexpr int F_S = 0, F_Z2 = 128, F_Z1 = 256, F_O = 384, F_U = 512;
constexpr unsigned WS_STATS = 131072, WS_GATE = 393216, WS_Z2N = 524288, WS_Z1 = 3407872, WS_O = 6291456, WS_U = 8388608;
constexpr unsigned WS_END = 14155776, WS_STAMP = 16u << 20;
constexpr int LDS_BYTES = 163840;
constexpr unsigned SPIN_CAP = 1u << 16;

template <int N> struct IC { static constexpr int v = N; };
template <int B, int E, class F> DI void sfor(F&& f) { if constexpr (B < E) { f(IC<B>{}); sfor<B + 1, E>(f); } }

DI unsigned rfl(unsigned x) { return __builtin_amdgcn_readfirstlane(x); }
DI void glds(srd_t srd, unsigned voff, unsigned soff, unsigned ldsdst) {
  asm volatile("s_nop 4\n\ts_mov_b32 m0, %0\n\ts_nop 0\n\tbuffer_load_dwordx4 %1, %2, %3 offen lds"
               :: "s"(ldsdst), "v"(voff), "s"(srd), "s"(soff) : "m0", "memory");
}
template <int N> DI void wait_vm() { asm volatile("s_waitcnt vmcnt(%0)" :: "n"(N) : "memory"); }
template <int N> DI void wait_bar() { asm volatile("s_waitcnt vmcnt(%0) lgkmcnt(0)\n\ts_barrier" :: "n"(N) : "memory"); }
DI void bar() { asm volatile("s_waitcnt lgkmcnt(0)\n\ts_barrier" ::: "memory"); }

DI bf16x8 lds128(unsigned a) { return *(const LAS bf16x8*)(uintptr_t)a; }
DI f32x4 lds128f(unsigned a) { return *(const LAS f32x4*)(uintptr_t)a; }
DI void lds_st128(unsigned a, f32x4 v) { *(LAS f32x4*)(uintptr_t)a = v; }
DI void lds_st64(unsigned a, u32x2 v) { *(LAS u32x2*)(uintptr_t)a = v; }
DI u32x2 lds64(unsigned a) { return *(const LAS u32x2*)(uintptr_t)a; }
DI u32x4 lds128u(unsigned a) { return *(const LAS u32x4*)(uintptr_t)a; }
DI s16x4 ldstr(unsigned a) { return __builtin_amdgcn_ds_read_tr16_b64_v4i16((LAS s16x4*)(uintptr_t)a); }
DI bf16x8 ldstr2(unsigned a0, unsigned a1) {
  s16x4 lo = ldstr(a0), hi = ldstr(a1);
  return __builtin_bit_cast(bf16x8, __builtin_shufflevector(lo, hi, 0, 1, 2, 3, 4, 5, 6, 7));
}
DI f32x4 mfma32(bf16x8 a, bf16x8 b, f32x4 c) { return __builtin_amdgcn_mfma_f32_16x16x32_bf16(a, b, c, 0, 0, 0); }
DI f32x4 mfma16(u32x2 a, u32x2 b, f32x4 c) {
  return __builtin_amdgcn_mfma_f32_16x16x16bf16_1k(__builtin_bit_cast(s16x4, a), __builtin_bit_cast(s16x4, b), c, 0, 0, 0);
}
DI unsigned pk2(float a, float b) { f32x2 p = {a, b}; return __builtin_bit_cast(unsigned, __builtin_convertvector(p, bf16x2)); }
DI u32x2 pk4(f32x4 v) { u32x2 r = {pk2(v[0], v[1]), pk2(v[2], v[3])}; return r; }
DI float bflo(unsigned u) { return __uint_as_float(u << 16); }
DI float bfhi(unsigned u) { return __uint_as_float(u & 0xffff0000u); }
DI float gelu_f(float x) {
  constexpr float C0 = -2.0f * 0.7978845608028654f * 1.4426950408889634f, C1 = C0 * 0.044715f;
  float p = __builtin_fmaf(x * x, C1, C0);
  float e = __builtin_amdgcn_exp2f(p * x);
  return x * __builtin_amdgcn_rcpf(1.0f + e);
}
template <int CTRL> DI float dpp_add(float x) {
  return x + __builtin_bit_cast(float, __builtin_amdgcn_update_dpp(0, __builtin_bit_cast(int, x), CTRL, 0xf, 0xf, true));
}
DI float red16(float x) {
  x = dpp_add<0xB1>(x);
  x = dpp_add<0x4E>(x);
  x = dpp_add<0x141>(x);
  x = dpp_add<0x140>(x);
  return x;
}
DI float xq_max(float x) {
  auto a = __builtin_amdgcn_permlane16_swap(__float_as_uint(x), __float_as_uint(x), false, false);
  x = fmaxf(__uint_as_float(a[0]), __uint_as_float(a[1]));
  auto b = __builtin_amdgcn_permlane32_swap(__float_as_uint(x), __float_as_uint(x), false, false);
  return fmaxf(__uint_as_float(b[0]), __uint_as_float(b[1]));
}
DI float xq_sum(float x) {
  auto a = __builtin_amdgcn_permlane16_swap(__float_as_uint(x), __float_as_uint(x), false, false);
  x = __uint_as_float(a[0]) + __uint_as_float(a[1]);
  auto b = __builtin_amdgcn_permlane32_swap(__float_as_uint(x), __float_as_uint(x), false, false);
  return __uint_as_float(b[0]) + __uint_as_float(b[1]);
}

DI void st128(srd_t s, unsigned off, u32x4 v) { __builtin_amdgcn_raw_buffer_store_b128(v, s, off, 0, 16); }
DI void st64(srd_t s, unsigned off, u32x2 v) { __builtin_amdgcn_raw_buffer_store_b64(v, s, off, 0, 16); }
DI u32x4 ld128(srd_t s, unsigned voff, unsigned soff) { return __builtin_amdgcn_raw_buffer_load_b128(s, voff, soff, 16); }
DI u32x2 ld64(srd_t s, unsigned voff, unsigned soff) { return __builtin_amdgcn_raw_buffer_load_b64(s, voff, soff, 16); }

DI void set_flag(char* ws, int idx, u64 tag) {
  __hip_atomic_store((GAS u64*)(ws + WS_FLAGS + (size_t)idx * FLAG_STRIDE), tag, __ATOMIC_RELAXED, __HIP_MEMORY_SCOPE_AGENT);
}
DI void wait_flags(char* ws, int idx, u64 tag) {
  GAS u64* p = (GAS u64*)(ws + WS_FLAGS + (size_t)(idx < 0 ? 0 : idx) * FLAG_STRIDE);
  for (unsigned spin = 0; spin < SPIN_CAP; ++spin) {
    u64 v = __hip_atomic_load(p, __ATOMIC_RELAXED, __HIP_MEMORY_SCOPE_AGENT);
    if (__all(idx < 0 || v == tag)) break;
    __builtin_amdgcn_s_sleep(1);
  }
  asm volatile("" ::: "memory");
}

DI void xmap(int p, int lane, int T2, int& R, int& c) {
  int mp = 4 * p + (lane >> 4), slot = lane & 15, m = (mp + T2) & 15, par = (slot >> 3) ^ (m >> 3);
  R = 2 * mp + par; c = (slot & 7) ^ ((m & 7) ^ (par << 1));
}
DI unsigned xaddr(int R, int T, int c) {
  int tau = (R + T) & 31, par = tau & 1, m = tau >> 1;
  return (unsigned)((R >> 1) * 256 + 16 * ((8 * par + c) ^ (8 * (m >> 3) + ((m & 7) ^ (par << 1)))));
}

struct ZSched { int n[2][32]; };
constexpr ZSched make_zsched() {
  ZSched z{};
  for (int cls = 0; cls < 2; ++cls) {
    int seq[256] = {}; int n = 0;
    seq[n++] = 100; seq[n++] = 100;
    if (cls == 0) { seq[n++] = 0; seq[n++] = 1; }
    seq[n++] = 101; seq[n++] = 101;
    if (cls == 0) { seq[n++] = 2; seq[n++] = 3; }
    seq[n++] = 102; seq[n++] = 102;
    for (int s = 0; s < 32; ++s) {
      int last = -1;
      for (int t = 0; t < n; ++t) if (seq[t] == s || (s < 16 && seq[t] == 100 + (s >> 1))) last = t;
      z.n[cls][s] = last < 0 ? 63 : n - 1 - last;
      if (s + 4 < 32 && cls == 0) seq[n++] = s + 4;
      if ((s & 1) == 0 && (s / 2 + 3) <= 7) { seq[n++] = 100 + s / 2 + 3; seq[n++] = 100 + s / 2 + 3; }
      if (s == 15) seq[n++] = 200;
    }
  }
  return z;
}
constexpr ZSched ZS = make_zsched();

#define STAMP(k) do {} while (0)

template <int PH>
__global__ __launch_bounds__(512, 2) void mk_kernel(const __hip_bfloat16* __restrict__ x_, const __hip_bfloat16* __restrict__ wqkv_,
                                                    const __hip_bfloat16* __restrict__ wo_, const __hip_bfloat16* __restrict__ win_,
                                                    const __hip_bfloat16* __restrict__ wsp_, const __hip_bfloat16* __restrict__ bsp_,
                                                    const __hip_bfloat16* __restrict__ wout_, const __hip_bfloat16* __restrict__ gw_,
                                                    __hip_bfloat16* __restrict__ out_, char* __restrict__ ws) {
  extern __shared__ __attribute__((aligned(16))) char smem[];
  const unsigned L0 = (unsigned)(uintptr_t)(LAS char*)smem;
  const int tid = threadIdx.x, lane = tid & 63;
  const int w = (int)rfl((unsigned)tid >> 6);
  const int c16 = lane & 15, q4 = lane >> 4;
  const int tq = (lane & 15) >> 2, tp = lane & 3;
  const int bid = blockIdx.x, xcd = bid & 7, rr = bid >> 3;

  const srd_t sX = __builtin_amdgcn_make_buffer_rsrc((void*)x_, 0, 2048 * 512 * 2, 0x00020000);
  const srd_t sWS = __builtin_amdgcn_make_buffer_rsrc((void*)ws, 0, WS_END, 0x00020000);
  const srd_t sWSP = __builtin_amdgcn_make_buffer_rsrc((void*)wsp_, 0, 2048 * 2048 * 2, 0x00020000);
  const u64 tag = __hip_atomic_load((GAS u64*)(ws + WS_GEN), __ATOMIC_RELAXED, __HIP_MEMORY_SCOPE_AGENT) + 1ull;
  STAMP(0);

  const int p2_q = xcd & 1, p2_i = 8 * (xcd >> 1) + (rr & 7), p2_J = rr >> 3;
  unsigned wsp_voff;
  { int R, c; xmap(w, lane, 0, R, c); wsp_voff = (unsigned)((64 * p2_i + R) * 4096 + p2_q * 2048 + c * 16); }
  auto wsp_issue = [&](int kk) { glds(sWSP, wsp_voff, (unsigned)kk * 128u, L0 + (unsigned)kk * 8192u + (unsigned)w * 1024u); };

  if constexpr (PH & 1) {
    const int i1 = 8 * (xcd & 1) + (rr & 7), cc = rr >> 3, cg = 2 * (xcd >> 1) + (cc & 1);
    if (cc < 2) {
      const int j = cg;
      const srd_t sWIN = __builtin_amdgcn_make_buffer_rsrc((void*)win_, 0, 512 * 1376 * 2, 0x00020000);
      constexpr unsigned LW = 131072, LSC = 161792;
      const int rp = w & 3, ch = w >> 2;
      unsigned xv0, xv1, wv_z2, wv_z1;
      { int R, c; xmap(w, lane, 0, R, c); xv0 = (unsigned)((128 * i1 + R) * 1024 + c * 16);
        xmap(w + 8, lane, 0, R, c); xv1 = (unsigned)((128 * i1 + R) * 1024 + c * 16); }
      { int n = 64 * (w < 6 ? w : 0) + lane, row = n / 12, ccx = n % 12, cb = (ccx >> 1) ^ ((row >> 3) & 1);
        int col = 96 * j + 16 * cb + 8 * (ccx & 1);
        int c2 = 688 + col; if (c2 > 1368) c2 = 1368;
        wv_z1 = (unsigned)(row * 2752 + col * 2); wv_z2 = (unsigned)(row * 2752 + c2 * 2); }
      auto issueX = [&](int kk) {
        glds(sX, xv0, (unsigned)kk * 128u, L0 + (unsigned)kk * 16384u + (unsigned)w * 1024u);
        glds(sX, xv1, (unsigned)kk * 128u, L0 + (unsigned)kk * 16384u + (unsigned)(w + 8) * 1024u);
      };
      auto issueW = [&](int s) {
        if (w < 6) glds(sWIN, s < 16 ? wv_z2 : wv_z1, (unsigned)(s & 15) * (32u * 2752u), L0 + LW + (unsigned)(s % 5) * 6144u + (unsigned)w * 1024u);
      };
      const bool gate_wave = (j == 7 && ch == 1);
      if (j == 7 && tid < 64) *(LAS u32x4*)(uintptr_t)(L0 + LSC + 1024 + tid * 16) = *(const u32x4*)(gw_ + tid * 8);
      issueX(0); issueW(0); issueW(1); issueX(1); issueW(2); issueW(3); issueX(2);

      unsigned xa_z[2][2], xa_p[2][2];
#pragma unroll
      for (int b = 0; b < 2; ++b)
#pragma unroll
        for (int ks = 0; ks < 2; ++ks) {
          xa_z[b][ks] = L0 + xaddr(32 * rp + 8 * (c16 >> 2) + 4 * b + (c16 & 3), 0, 4 * ks + q4);
          xa_p[b][ks] = L0 + xaddr(32 * rp + 2 * c16 + b, 0, 4 * ks + q4);
        }
      unsigned wa[3];
#pragma unroll
      for (int cb = 0; cb < 3; ++cb) wa[cb] = L0 + LW + (unsigned)((8 * q4 + tq) * 192 + 32 * ((3 * ch + cb) ^ (q4 & 1)) + 8 * tp);

      f32x4 az[2][3], a1[3][2];
#pragma unroll
      for (int b = 0; b < 2; ++b)
#pragma unroll
        for (int cb = 0; cb < 3; ++cb) { az[b][cb] = f32x4{0, 0, 0, 0}; a1[cb][b] = f32x4{0, 0, 0, 0}; }
      const int nv = j < 7 ? 3 : (ch == 0 ? 1 : 0);

      sfor<0, 32>([&](auto S_) {
        constexpr int s = decltype(S_)::v;
        if (w < 6) wait_bar<ZS.n[0][s]>(); else wait_bar<ZS.n[1][s]>();
        if constexpr (s + 4 < 32) issueW(s + 4);
        if constexpr ((s & 1) == 0 && (s / 2 + 3) <= 7) issueX(s / 2 + 3);
        if constexpr (s == 20) { if (tid == 0) set_flag(ws, F_S + i1 * 8 + j, tag); }
        constexpr unsigned xo = (unsigned)((s & 15) >> 1) * 16384u, wo = (unsigned)(s % 5) * 6144u;
        constexpr int ks = s & 1;
        if constexpr (s < 16) {
          bf16x8 xf0 = lds128(xa_z[0][ks] + xo), xf1 = lds128(xa_z[1][ks] + xo);
#pragma unroll
          for (int cb = 0; cb < 3; ++cb) {
            bf16x8 wf = ldstr2(wa[cb] + wo, wa[cb] + wo + 768);
            az[0][cb] = mfma32(xf0, wf, az[0][cb]);
            az[1][cb] = mfma32(xf1, wf, az[1][cb]);
          }
        } else {
          bf16x8 xf0 = lds128(xa_p[0][ks] + xo), xf1 = lds128(xa_p[1][ks] + xo);
          if (gate_wave) {
            bf16x8 gf = lds128(L0 + LSC + 1024 + (unsigned)((s - 16) * 64 + q4 * 16));
            a1[0][0] = mfma32(gf, xf0, a1[0][0]);
            a1[0][1] = mfma32(gf, xf1, a1[0][1]);
          } else {
#pragma unroll
            for (int cb = 0; cb < 3; ++cb) {
              bf16x8 wf = ldstr2(wa[cb] + wo, wa[cb] + wo + 768);
              a1[cb][0] = mfma32(wf, xf0, a1[cb][0]);
              a1[cb][1] = mfma32(wf, xf1, a1[cb][1]);
            }
          }
        }
        if constexpr (s == 15) {
          STAMP(1);
          float mean[8], m2[8];
#pragma unroll
          for (int b = 0; b < 2; ++b)
#pragma unroll
            for (int cb = 0; cb < 3; ++cb)
#pragma unroll
              for (int r = 0; r < 4; ++r) az[b][cb][r] = gelu_f(az[b][cb][r]);
          const float inv_n = nv > 0 ? 1.0f / (float)(16 * nv) : 0.f;
#pragma unroll
          for (int b = 0; b < 2; ++b)
#pragma unroll
            for (int r = 0; r < 4; ++r) {
              float t = az[b][0][r];
              if (nv == 3) t += az[b][1][r] + az[b][2][r];
              float mu = red16(t) * inv_n;
              float d0 = az[b][0][r] - mu, v = d0 * d0;
              if (nv == 3) { float d1 = az[b][1][r] - mu, d2 = az[b][2][r] - mu; v += d1 * d1 + d2 * d2; }
              mean[4 * b + r] = mu; m2[4 * b + r] = red16(v);
            }
          float sm = mean[0], sv = m2[0];
#pragma unroll
          for (int k = 1; k < 8; ++k) { if (c16 == k) { sm = mean[k]; sv = m2[k]; } }
          unsigned so = WS_STATS + (unsigned)(((i1 * 128 + 32 * rp + 8 * q4 + c16) * 16 + 2 * j + ch) * 8);
          if (c16 >= 8) so = 0xfffffff0u;
          u32x2 sv2 = {__float_as_uint(sm), __float_as_uint(sv)};
          st64(sWS, so, sv2);
        }
      });
      STAMP(2);
      wait_flags(ws, lane < 8 ? F_S + i1 * 8 + lane : -1, tag);
      {
        const int row = 32 * rp + (lane >> 1), hf = lane & 1;
        const unsigned so = WS_STATS + (unsigned)(((i1 * 128 + row) * 16 + 8 * hf) * 8);
        u32x4 p[4];
#pragma unroll
        for (int k = 0; k < 4; ++k) p[k] = ld128(sWS, so + 16 * k, 0);
        float mu[8], mm[8], nn[8];
#pragma unroll
        for (int k = 0; k < 4; ++k) { mu[2 * k] = __uint_as_float(p[k][0]); mm[2 * k] = __uint_as_float(p[k][1]); mu[2 * k + 1] = __uint_as_float(p[k][2]); mm[2 * k + 1] = __uint_as_float(p[k][3]); }
#pragma unroll
        for (int k = 0; k < 8; ++k) nn[k] = 48.f;
        if (hf) { nn[6] = 16.f; nn[7] = 0.f; mu[7] = 0.f; mm[7] = 0.f; }
        float s1 = 0.f;
#pragma unroll
        for (int k = 0; k < 8; ++k) s1 += nn[k] * mu[k];
        s1 = dpp_add<0xB1>(s1);
        const float mean = s1 * (1.0f / 688.0f);
        float s2 = 0.f;
#pragma unroll
        for (int k = 0; k < 8; ++k) { float d = mu[k] - mean; s2 += mm[k] + nn[k] * d * d; }
        s2 = dpp_add<0xB1>(s2);
        const float rstd = __builtin_amdgcn_rsqf(s2 * (1.0f / 688.0f) + 1e-5f);
        const unsigned sc = L0 + LSC + (unsigned)(w & 3) * 256u;
        if (hf == 0) lds_st64(sc + (unsigned)(lane >> 1) * 8u, u32x2{__float_as_uint(mean), __float_as_uint(rstd)});
        asm volatile("s_waitcnt lgkmcnt(0)" ::: "memory");
        f32x4 st[4];
#pragma unroll
        for (int k = 0; k < 4; ++k) st[k] = lds128f(sc + (unsigned)(8 * q4) * 8u + 16u * k);
#pragma unroll
        for (int cb = 0; cb < 3; ++cb) {
          u32x4 o;
#pragma unroll
          for (int b = 0; b < 2; ++b) {
            f32x4 v;
#pragma unroll
            for (int r = 0; r < 4; ++r) { const int k = 4 * b + r; v[r] = (az[b][cb][r] - st[k >> 1][2 * (k & 1)]) * st[k >> 1][2 * (k & 1) + 1]; }
            u32x2 pk = pk4(v); o[2 * b] = pk[0]; o[2 * b + 1] = pk[1];
          }
          if (cb < nv) st128(sWS, WS_Z2N + (unsigned)((((4 * i1 + rp) * 44 + 6 * j + 3 * ch + cb) * 64 + lane) * 16), o);
        }
      }
      if (gate_wave) {
        float g0 = __builtin_amdgcn_rcpf(1.0f + __builtin_amdgcn_exp2f(-1.4426950408889634f * a1[0][0][0]));
        float g1 = __builtin_amdgcn_rcpf(1.0f + __builtin_amdgcn_exp2f(-1.4426950408889634f * a1[0][1][0]));
        unsigned go = WS_GATE + (unsigned)((128 * i1 + 32 * rp + 2 * c16) * 4);
        if (q4 != 0) go = 0xfffffff0u;
        st64(sWS, go, u32x2{__float_as_uint(g0), __float_as_uint(g1)});
      } else {
#pragma unroll
        for (int cb = 0; cb < 3; ++cb) {
          u32x4 o;
#pragma unroll
          for (int b = 0; b < 2; ++b) {
            f32x4 v;
#pragma unroll
            for (int r = 0; r < 4; ++r) v[r] = gelu_f(a1[cb][b][r]);
            u32x2 pk = pk4(v); o[2 * b] = pk[0]; o[2 * b + 1] = pk[1];
          }
          if (cb < nv) st128(sWS, WS_Z1 + (unsigned)((((4 * i1 + rp) * 44 + 6 * j + 3 * ch + cb) * 64 + lane) * 16), o);
        }
      }
      wait_bar<0>();
      if (tid == 0) { set_flag(ws, F_Z2 + i1 * 8 + j, tag); set_flag(ws, F_Z1 + i1 * 8 + j, tag); }
      STAMP(3);
    } else {
      const int hg = cg;
      const srd_t sWQ = __builtin_amdgcn_make_buffer_rsrc((void*)wqkv_, 0, 512 * 1536 * 2, 0x00020000);
      constexpr unsigned SLOT = 43008, LWQ = 18432, LSC = 131072;
      const int h = w & 3, th = w >> 2;
      unsigned xv0, xv1, xvh, wv;
      { int R, c, t;
        xmap(2 + w, lane, 8, R, c); xv0 = (unsigned)((128 * i1 - 16 + R) * 1024 + c * 16);
        xmap(10 + w, lane, 8, R, c); xv1 = (unsigned)((128 * i1 - 16 + R) * 1024 + c * 16);
        xmap(w & 1, lane, 8, R, c); t = 128 * i1 - 16 + R; if (t < 0) t = 0; xvh = (unsigned)(t * 1024 + c * 16); }
      { int row = 8 * w + (lane >> 3), cx = lane & 7, f = ((row >> 1) & 1) | ((row >> 2) & 2);
        int col = 64 * hg + 16 * ((cx >> 1) ^ f) + 8 * (cx & 1);
        wv = (unsigned)(row * 3072 + col * 2); }
      const bool halo_loader = (w == 4 || w == 5);
      auto issue = [&](int s) {
        const unsigned base = L0 + (unsigned)(s % 3) * SLOT;
        glds(sX, xv0, (unsigned)s * 128u, base + (unsigned)(2 + w) * 1024u);
        glds(sX, xv1, (unsigned)s * 128u, base + (unsigned)(10 + w) * 1024u);
#pragma unroll
        for (int sg = 0; sg < 3; ++sg) glds(sWQ, wv, (unsigned)s * (64u * 3072u) + (unsigned)sg * 1024u, base + LWQ + (unsigned)sg * 8192u + (unsigned)w * 1024u);
        if (halo_loader) glds(sX, xvh, (unsigned)s * 128u, base + (unsigned)(w & 1) * 1024u);
      };
      issue(0); issue(1); issue(2);
      unsigned xa[2][2], xh[2];
#pragma unroll
      for (int ks = 0; ks < 2; ++ks) {
#pragma unroll
        for (int par = 0; par < 2; ++par) xa[par][ks] = L0 + xaddr(16 + 64 * th + 2 * c16 + par, 16, 4 * ks + q4);
        xh[ks] = L0 + xaddr(c16, 16, 4 * ks + q4);
      }
      const unsigned wa = L0 + LWQ + (unsigned)((8 * q4 + tq) * 128 + 32 * (h ^ ((tq >> 1) | ((q4 & 1) << 1))) + 8 * tp);
      f32x4 aq[4], ak[5], av[5];
#pragma unroll
      for (int n = 0; n < 4; ++n) aq[n] = f32x4{0, 0, 0, 0};
#pragma unroll
      for (int n = 0; n < 5; ++n) { ak[n] = f32x4{0, 0, 0, 0}; av[n] = f32x4{0, 0, 0, 0}; }
      sfor<0, 8>([&](auto S_) {
        constexpr int s = decltype(S_)::v;
        constexpr int younger = s == 0 ? 2 : (s == 7 ? 0 : 1);
        if (halo_loader) wait_bar<6 * younger>(); else wait_bar<5 * younger>();
        if constexpr (s >= 1 && s + 2 < 8) issue(s + 2);
        constexpr unsigned so = (unsigned)(s % 3) * SLOT;
#pragma unroll
        for (int ks = 0; ks < 2; ++ks) {
          bf16x8 wq = ldstr2(wa + so + ks * 4096, wa + so + ks * 4096 + 512);
          bf16x8 wk = ldstr2(wa + so + 8192 + ks * 4096, wa + so + 8192 + ks * 4096 + 512);
          bf16x8 wvv = ldstr2(wa + so + 16384 + ks * 4096, wa + so + 16384 + ks * 4096 + 512);
#pragma unroll
          for (int n = 0; n < 4; ++n) {
            bf16x8 xf = lds128(xa[n & 1][ks] + so + (unsigned)(n >> 1) * 4096u);
            aq[n] = mfma32(wq, xf, aq[n]);
            ak[n] = mfma32(wk, xf, ak[n]);
            av[n] = mfma32(xf, wvv, av[n]);
          }
          if (th == 0) {
            bf16x8 xf = lds128(xh[ks] + so);
            ak[4] = mfma32(wk, xf, ak[4]);
            av[4] = mfma32(xf, wvv, av[4]);
          }
        }
      });
      STAMP(1);
      u32x2 qb[4], kb[5], vb[5];
#pragma unroll
      for (int n = 0; n < 4; ++n) qb[n] = pk4(aq[n]);
#pragma unroll
      for (int n = 0; n < 5; ++n) { kb[n] = pk4(ak[n]); vb[n] = pk4(av[n]); }
      const unsigned ex = L0 + LSC + (unsigned)h * 2048u + (unsigned)lane * 8u;
      if (th == 0) { lds_st64(ex, kb[2]); lds_st64(ex + 512, kb[3]); lds_st64(ex + 1024, vb[2]); lds_st64(ex + 1536, vb[3]); }
      bar();
      u32x2 kp[2], vp[2];
      if (th == 0) { kp[0] = kb[4]; kp[1] = kb[4]; vp[0] = vb[4]; vp[1] = vb[4]; }
      else { kp[0] = lds64(ex); kp[1] = lds64(ex + 512); vp[0] = lds64(ex + 1024); vp[1] = lds64(ex + 1536); }
      constexpr float NEG = -1e30f;
      f32x4 bias_c, bias_p, bias_h[2];
      unsigned mc[2], mp_[2], mh[2][2];
      {
        unsigned vc = 0, vpv = 0, vh0 = 0, vh1 = 0;
#pragma unroll
        for (int r = 0; r < 4; ++r) {
          const int kap = 4 * q4 + r;
          const bool c_ok = (kap <= c16) && (kap >= c16 - 7);
          const bool p_ok = kap >= c16 + 9;
          const bool h0 = (i1 > 0) && ((kap & 1) == 0) && (kap >= 2 * c16 + 2);
          const bool h1 = (i1 > 0) && ((kap & 1) == 1) && (kap >= 2 * c16 + 3);
          bias_c[r] = c_ok ? 0.f : NEG; bias_p[r] = p_ok ? 0.f : NEG; bias_h[0][r] = h0 ? 0.f : NEG; bias_h[1][r] = h1 ? 0.f : NEG;
          vc |= (unsigned)c_ok << r; vpv |= (unsigned)p_ok << r; vh0 |= (unsigned)h0 << r; vh1 |= (unsigned)h1 << r;
        }
        auto mk = [](unsigned v, int k) { return ((v >> (2 * k)) & 1u ? 0xffffu : 0u) | ((v >> (2 * k + 1)) & 1u ? 0xffff0000u : 0u); };
#pragma unroll
        for (int k = 0; k < 2; ++k) { mc[k] = mk(vc, k); mp_[k] = mk(vpv, k); mh[0][k] = mk(vh0, k); mh[1][k] = mk(vh1, k); }
      }
      constexpr float C2 = 0.25f * 1.4426950408889634f;
#pragma unroll
      for (int n = 0; n < 4; ++n) {
        const bool first = n < 2;
        const bool use_h = first && th == 0;
        const u32x2 kprev = first ? kp[n & 1] : kb[n - 2], vprev = first ? vp[n & 1] : vb[n - 2];
        const f32x4 bp = use_h ? bias_h[n & 1] : bias_p;
        const unsigned mp0 = use_h ? mh[n & 1][0] : mp_[0], mp1 = use_h ? mh[n & 1][1] : mp_[1];
        f32x4 sp = mfma16(kprev, qb[n], bp), sc = mfma16(kb[n], qb[n], bias_c);
        f32x4 sm;
#pragma unroll
        for (int r = 0; r < 4; ++r) sm[r] = fmaxf(sp[r], sc[r]);
        float m = xq_max(fmaxf(fmaxf(sm[0], sm[1]), fmaxf(sm[2], sm[3])));
        f32x4 p;
#pragma unroll
        for (int r = 0; r < 4; ++r) p[r] = __builtin_amdgcn_exp2f((sm[r] - m) * C2);
        const float inv = __builtin_amdgcn_rcpf(xq_sum((p[0] + p[1]) + (p[2] + p[3])));
#pragma unroll
        for (int r = 0; r < 4; ++r) p[r] *= inv;
        const u32x2 pp = pk4(p);
        u32x4 pb = {pp[0] & mp0, pp[1] & mp1, pp[0] & mc[0], pp[1] & mc[1]};
        u32x4 va = {vprev[0], vprev[1], vb[n][0], vb[n][1]};
        f32x4 o = mfma32(__builtin_bit_cast(bf16x8, va), __builtin_bit_cast(bf16x8, pb), f32x4{0, 0, 0, 0});
        const int tbl = 4 * th + n;
        lds_st64(L0 + LSC + 8192 + (unsigned)(((tbl * 2 + (h >> 1)) * 64 + lane) * 16 + (h & 1) * 8), pk4(o));
      }
      bar();
#pragma unroll
      for (int k = 0; k < 2; ++k) {
        const int f = w + 8 * k;
        u32x4 v = lds128u(L0 + LSC + 8192 + (unsigned)(f * 1024 + lane * 16));
        st128(sWS, WS_O + (unsigned)((((8 * i1 + (f >> 1)) * 16 + 2 * hg + (f & 1)) * 64 + lane) * 16), v);
      }
      wait_bar<0>();
      if (tid == 0) set_flag(ws, F_O + i1 * 8 + hg, tag);
      STAMP(3);
    }
  }

  if constexpr (PH & 2) {
    const int q = p2_q, i2 = p2_i, J = p2_J;
#pragma unroll
    for (int kk = 0; kk < 16; ++kk) wsp_issue(kk);
    const int fb = w < 4 ? 12 * J + 2 * w : 12 * J + 4 + w;
    const int nf = w < 4 ? (fb + 1 <= 42 ? 2 : 1) : (fb <= 42 ? 1 : 0);
    wait_flags(ws, lane < 16 ? F_Z2 + (8 * q + (lane >> 1)) * 8 + 2 * J + (lane & 1) : (lane < 18 ? F_Z1 + (i2 >> 1) * 8 + 2 * J + (lane & 1) : -1), tag);
    STAMP(4);
    unsigned ta[2][2];
#pragma unroll
    for (int par = 0; par < 2; ++par)
#pragma unroll
      for (int ks = 0; ks < 2; ++ks) ta[par][ks] = L0 + xaddr(2 * c16 + par, 0, 4 * ks + q4);
    const unsigned zv = WS_Z2N + (unsigned)(((32 * q) * 44 + fb) * 1024 + lane * 16);
    f32x4 acc[2][4];
#pragma unroll
    for (int f = 0; f < 2; ++f)
#pragma unroll
      for (int t = 0; t < 4; ++t) acc[f][t] = f32x4{0, 0, 0, 0};
    auto body = [&](auto NF_) {
      constexpr int NF = decltype(NF_)::v;
      constexpr int D = 8;
      u32x4 zb[D][NF];
      sfor<0, D>([&](auto S_) { constexpr int s = decltype(S_)::v;
#pragma unroll
        for (int f = 0; f < NF; ++f) zb[s][f] = ld128(sWS, zv + f * 1024, (unsigned)s * 45056u); });
      wait_bar<D * NF>();
      sfor<0, 32>([&](auto S_) {
        constexpr int s = decltype(S_)::v;
        constexpr unsigned ko = (unsigned)(s >> 1) * 8192u;
        bf16x8 tf[4];
#pragma unroll
        for (int t = 0; t < 4; ++t) tf[t] = lds128(ta[t & 1][s & 1] + ko + (unsigned)(t >> 1) * 4096u);
#pragma unroll
        for (int f = 0; f < NF; ++f) {
          bf16x8 a = __builtin_bit_cast(bf16x8, zb[s % D][f]);
#pragma unroll
          for (int t = 0; t < 4; ++t) acc[f][t] = mfma32(a, tf[t], acc[f][t]);
        }
        if constexpr (s + D < 32) {
#pragma unroll
          for (int f = 0; f < NF; ++f) zb[s % D][f] = ld128(sWS, zv + f * 1024, (unsigned)(s + D) * 45056u);
        }
      });
    };
    if (nf == 2) body(IC<2>{}); else if (nf == 1) body(IC<1>{}); else wait_bar<0>();
    STAMP(5);
    if (nf > 0) {
      float bs[4] = {0.f, 0.f, 0.f, 0.f};
      if (q == 0) {
#pragma unroll
        for (int g2 = 0; g2 < 2; ++g2) {
          unsigned bb = *(const unsigned*)(bsp_ + 64 * i2 + 32 * g2 + 2 * c16);
          bs[2 * g2] = bflo(bb); bs[2 * g2 + 1] = bfhi(bb);
        }
      }
      u32x2 ub[2][4];
#pragma unroll
      for (int f = 0; f < 2; ++f) {
#pragma unroll
        for (int g2 = 0; g2 < 2; ++g2) {
          u32x4 z = u32x4{0, 0, 0, 0};
          if (f < nf) z = ld128(sWS, WS_Z1 + (unsigned)((((2 * i2 + g2) * 44 + fb + f) * 64 + lane) * 16), 0);
#pragma unroll
          for (int par = 0; par < 2; ++par) {
            const int t = 2 * g2 + par;
            f32x4 v;
            v[0] = bflo(z[2 * par]) * (acc[f][t][0] + bs[t]); v[1] = bfhi(z[2 * par]) * (acc[f][t][1] + bs[t]);
            v[2] = bflo(z[2 * par + 1]) * (acc[f][t][2] + bs[t]); v[3] = bfhi(z[2 * par + 1]) * (acc[f][t][3] + bs[t]);
            ub[f][t] = pk4(v);
          }
        }
      }
#pragma unroll
      for (int t = 0; t < 4; ++t) {
        const unsigned uo = WS_U + (unsigned)((((q * 128 + 4 * i2 + t) * 22 + (fb >> 1)) * 64 + lane) * 16);
        if (w < 4) st128(sWS, uo, u32x4{ub[0][t][0], ub[0][t][1], ub[1][t][0], ub[1][t][1]});
        else st64(sWS, uo + (unsigned)(fb & 1) * 8u, ub[0][t]);
      }
    }
    wait_bar<0>();
    if (tid == 0) set_flag(ws, F_U + (i2 * 4 + J) * 2 + q, tag);
    STAMP(6);
  }

  if constexpr (PH & 4) {
    const int i3 = 4 * xcd + (rr & 3), j3 = rr >> 2;
    const srd_t sWO = __builtin_amdgcn_make_buffer_rsrc((void*)wo_, 0, 512 * 512 * 2, 0x00020000);
    const srd_t sWOUT = __builtin_amdgcn_make_buffer_rsrc((void*)wout_, 0, 688 * 512 * 2, 0x00020000);
    {
      const int row = 8 * w + (lane >> 3), cx = lane & 7;
      const unsigned bv = (unsigned)(row * 1024 + (64 * j3 + 16 * ((cx >> 1) ^ ((row >> 1) & 3)) + 8 * (cx & 1)) * 2);
#pragma unroll
      for (int n = 0; n < 8; ++n) glds(sWO, bv, (unsigned)n * 65536u, L0 + (unsigned)(n * 8 + w) * 1024u);
#pragma unroll
      for (int n = 0; n < 11; ++n) glds(sWOUT, bv, (unsigned)n * 65536u, L0 + 65536u + (unsigned)(n * 8 + w) * 1024u);
    }
    const int t2 = w & 1, kq = w >> 1;
    wait_flags(ws, lane < 8 ? F_O + (i3 >> 1) * 8 + lane : (lane == 8 ? F_Z1 + (i3 >> 1) * 8 + 7 : -1), tag);
    const unsigned wb = L0 + (unsigned)((4 * q4 + tq) * 128 + 32 * (2 * (q4 & 1) + (tq >> 1)) + 8 * tp);
    f32x4 aa[4][2], ab[4][2];
#pragma unroll
    for (int cb = 0; cb < 4; ++cb)
#pragma unroll
      for (int t = 0; t < 2; ++t) { aa[cb][t] = f32x4{0, 0, 0, 0}; ab[cb][t] = f32x4{0, 0, 0, 0}; }
    u32x4 of[4][2];
#pragma unroll
    for (int k = 0; k < 4; ++k)
#pragma unroll
      for (int t = 0; t < 2; ++t) of[k][t] = ld128(sWS, WS_O + (unsigned)((((4 * i3 + 2 * t2 + t) * 16 + 4 * kq + k) * 64 + lane) * 16), 0);
    const u32x2 gg = ld64(sWS, WS_GATE + (unsigned)((64 * i3 + 32 * t2 + 2 * c16) * 4), 0);
    wait_bar<0>();
    STAMP(7);
#pragma unroll
    for (int k = 0; k < 4; ++k) {
      const unsigned base = (unsigned)(4 * kq + k) * 4096u;
#pragma unroll
      for (int cb = 0; cb < 4; ++cb) {
        bf16x8 wf = ldstr2((wb ^ (unsigned)(cb << 5)) + base, (wb ^ (unsigned)(cb << 5)) + base + 2048);
#pragma unroll
        for (int t = 0; t < 2; ++t) aa[cb][t] = mfma32(wf, __builtin_bit_cast(bf16x8, of[k][t]), aa[cb][t]);
      }
    }
    wait_flags(ws, lane < 8 ? F_U + i3 * 8 + lane : -1, tag);
    if (bid == 0 && w == 7) {
#pragma unroll
      for (int k = 0; k < 4; ++k) wait_flags(ws, F_Z1 + 64 * k + lane, tag);
      if (lane == 0) __hip_atomic_store((GAS u64*)(ws + WS_GEN), tag, __ATOMIC_RELAXED, __HIP_MEMORY_SCOPE_AGENT);
    }
    STAMP(8);
    const int p0 = kq < 2 ? 6 * kq : 12 + 5 * (kq - 2), np = kq < 2 ? 6 : 5;
    u32x4 uf[6][2][2];
#pragma unroll
    for (int k = 0; k < 6; ++k)
#pragma unroll
      for (int qq = 0; qq < 2; ++qq)
#pragma unroll
        for (int t = 0; t < 2; ++t) {
          unsigned uo = WS_U + (unsigned)((((qq * 128 + 4 * i3 + 2 * t2 + t) * 22 + p0 + k) * 64 + lane) * 16);
          if (k == 5 && np == 5) uo = 0xfffffff0u;
          uf[k][qq][t] = ld128(sWS, uo, 0);
        }
#pragma unroll
    for (int k = 0; k < 6; ++k) {
      const int pk = (k == 5 && np == 5) ? p0 : p0 + k;
      const unsigned base = 65536u + (unsigned)pk * 4096u;
#pragma unroll
      for (int cb = 0; cb < 4; ++cb) {
        bf16x8 wf = ldstr2((wb ^ (unsigned)(cb << 5)) + base, (wb ^ (unsigned)(cb << 5)) + base + 2048);
#pragma unroll
        for (int qq = 0; qq < 2; ++qq)
#pragma unroll
          for (int t = 0; t < 2; ++t) ab[cb][t] = mfma32(wf, __builtin_bit_cast(bf16x8, uf[k][qq][t]), ab[cb][t]);
      }
    }
    bar();
    constexpr unsigned RP = 272;
#pragma unroll
    for (int t = 0; t < 2; ++t) {
      const float g = __uint_as_float(gg[t]), g1 = 1.0f - g;
      const unsigned ro = L0 + (unsigned)(kq * 64 + 32 * t2 + 2 * c16 + t) * RP + (unsigned)q4 * 16u;
#pragma unroll
      for (int cb = 0; cb < 4; ++cb) {
        f32x4 v;
#pragma unroll
        for (int r = 0; r < 4; ++r) v[r] = g * aa[cb][t][r] + g1 * ab[cb][t][r];
        lds_st128(ro + (unsigned)cb * 64u, v);
      }
    }
    bar();
    {
      const int tok = tid >> 3, chn = tid & 7;
      f32x4 s0 = f32x4{0, 0, 0, 0}, s1 = f32x4{0, 0, 0, 0};
#pragma unroll
      for (int k = 0; k < 4; ++k) {
        const unsigned ro = L0 + (unsigned)(k * 64 + tok) * RP + (unsigned)chn * 32u;
        s0 += lds128f(ro); s1 += lds128f(ro + 16);
      }
      u32x2 a = pk4(s0), b = pk4(s1);
      *(u32x4*)(out_ + (size_t)(64 * i3 + tok) * 512 + 64 * j3 + 8 * chn) = u32x4{a[0], a[1], b[0], b[1]};
    }
    STAMP(9);
  }
}

extern "C" void kernel_launch(void* const* d_in, const int* in_sizes, int n_in, void* d_out, int out_size, void* d_ws, size_t ws_size,
                              hipStream_t stream) {
  const __hip_bfloat16* x = (const __hip_bfloat16*)d_in[0];
  const __hip_bfloat16* wqkv = (const __hip_bfloat16*)d_in[1];
  const __hip_bfloat16* wo = (const __hip_bfloat16*)d_in[2];
  const __hip_bfloat16* win = (const __hip_bfloat16*)d_in[4];
  const __hip_bfloat16* wsp = (const __hip_bfloat16*)d_in[5];
  const __hip_bfloat16* bsp = (const __hip_bfloat16*)d_in[6];
  const __hip_bfloat16* wout = (const __hip_bfloat16*)d_in[7];
  const __hip_bfloat16* gw = (const __hip_bfloat16*)d_in[8];
  (void)hipFuncSetAttribute((const void*)mk_kernel<7>, hipFuncAttributeMaxDynamicSharedMemorySize, LDS_BYTES);
  mk_kernel<7><<<256, 512, LDS_BYTES, stream>>>(x, wqkv, wo, win, wsp, bsp, wout, gw, (__hip_bfloat16*)d_out, (char*)d_ws);
}
```

```cpp
#include <hip/hip_runtime.h>
#include <hip/hip_bf16.h>
#include <stdint.h>

#define DI __device__ __forceinline__
#define GAS __attribute__((address_space(1)))
#define LAS __attribute__((address_space(3)))
typedef __attribute__((ext_vector_type(8))) __bf16 bf16x8;
typedef __attribute__((ext_vector_type(2))) __bf16 bf16x2;
typedef __attribute__((ext_vector_type(4))) short s16x4;
typedef __attribute__((ext_vector_type(4))) float f32x4;
typedef __attribute__((ext_vector_type(2))) float f32x2;
typedef __attribute__((ext_vector_type(4))) unsigned u32x4;
typedef __attribute__((ext_vector_type(2))) unsigned u32x2;
typedef unsigned long long u64;
typedef __amdgpu_buffer_rsrc_t srd_t;

constexpr unsigned WS_GEN = 0, WS_FLAGS = 4096, FLAG_STRIDE = 128;
constexpr int F_Z2 = 128, F_Z1 = 256, F_O = 384, F_U = 512;
constexpr unsigned WS_GATE = 131072, WS_Z2N = 524288, WS_Z1 = 3407872, WS_O = 6291456, WS_U = 8388608, WS_STATS = 14155776;
constexpr unsigned WS_END = WS_STATS + 524288, WS_STAMP = 16u << 20;
constexpr int LDS_BYTES = 163840;
constexpr unsigned SPIN_CAP = 1u << 16;
constexpr unsigned OOB = 0x7fff0000u;

template <int N> struct IC { static constexpr int v = N; };
template <int B, int E, class F> DI void sfor(F&& f) { if constexpr (B < E) { f(IC<B>{}); sfor<B + 1, E>(f); } }
#define SCHED() __builtin_amdgcn_sched_barrier(0)

DI unsigned rfl(unsigned x) { return __builtin_amdgcn_readfirstlane(x); }
DI void glds(srd_t srd, unsigned voff, unsigned soff, unsigned ldsdst) {
  asm volatile("s_mov_b32 m0, %0\n\ts_nop 3\n\tbuffer_load_dwordx4 %1, %2, %3 offen lds"
               :: "s"(ldsdst), "v"(voff), "s"(srd), "s"(soff) : "m0", "memory");
}
DI void glds4(srd_t srd, unsigned voff, unsigned soff, unsigned ldsdst) {
  asm volatile("s_mov_b32 m0, %0\n\ts_nop 3\n\tbuffer_load_dword %1, %2, %3 offen lds"
               :: "s"(ldsdst), "v"(voff), "s"(srd), "s"(soff) : "m0", "memory");
}
template <int N> DI void wait_vm() { asm volatile("s_waitcnt vmcnt(%0)" :: "n"(N) : "memory"); }
template <int N> DI void wait_bar() { asm volatile("s_waitcnt vmcnt(%0) lgkmcnt(0)\n\ts_barrier" :: "n"(N) : "memory"); }
DI void bar() { asm volatile("s_waitcnt lgkmcnt(0)\n\ts_barrier" ::: "memory"); }
DI void ald128(u32x4& d, srd_t s, unsigned voff, unsigned soff) {
  asm volatile("s_nop 4\n\tbuffer_load_dwordx4 %0, %1, %2, %3 offen sc1" : "=v"(d) : "v"(voff), "s"(s), "s"(soff) : "memory");
}
DI void ald64(u32x2& d, srd_t s, unsigned voff, unsigned soff) {
  asm volatile("s_nop 4\n\tbuffer_load_dwordx2 %0, %1, %2, %3 offen sc1" : "=v"(d) : "v"(voff), "s"(s), "s"(soff) : "memory");
}
DI void ald32(unsigned& d, srd_t s, unsigned voff, unsigned soff) {
  asm volatile("s_nop 4\n\tbuffer_load_dword %0, %1, %2, %3 offen" : "=v"(d) : "v"(voff), "s"(s), "s"(soff) : "memory");
}
template <class T> DI void pin(T& x) { asm volatile("" : "+v"(x)); }

DI bf16x8 lds128(unsigned a) { return *(const LAS bf16x8*)(uintptr_t)a; }
DI f32x4 lds128f(unsigned a) { return *(const LAS f32x4*)(uintptr_t)a; }
DI void lds_st128(unsigned a, f32x4 v) { *(LAS f32x4*)(uintptr_t)a = v; }
DI void lds_st64(unsigned a, u32x2 v) { *(LAS u32x2*)(uintptr_t)a = v; }
DI u32x2 lds64(unsigned a) { return *(const LAS u32x2*)(uintptr_t)a; }
DI u32x4 lds128u(unsigned a) { return *(const LAS u32x4*)(uintptr_t)a; }
DI s16x4 ldstr(unsigned a) { return __builtin_amdgcn_ds_read_tr16_b64_v4i16((LAS s16x4*)(uintptr_t)a); }
DI bf16x8 ldstr2(unsigned a0, unsigned a1) {
  s16x4 lo = ldstr(a0), hi = ldstr(a1);
  return __builtin_bit_cast(bf16x8, __builtin_shufflevector(lo, hi, 0, 1, 2, 3, 4, 5, 6, 7));
}
DI f32x4 mfma32(bf16x8 a, bf16x8 b, f32x4 c) { return __builtin_amdgcn_mfma_f32_16x16x32_bf16(a, b, c, 0, 0, 0); }
DI f32x4 mfma16(u32x2 a, u32x2 b, f32x4 c) {
  return __builtin_amdgcn_mfma_f32_16x16x16bf16_1k(__builtin_bit_cast(s16x4, a), __builtin_bit_cast(s16x4, b), c, 0, 0, 0);
}
DI bf16x8 asbf(u32x4 v) { return __builtin_bit_cast(bf16x8, v); }
DI unsigned pk2(float a, float b) { f32x2 p = {a, b}; return __builtin_bit_cast(unsigned, __builtin_convertvector(p, bf16x2)); }
DI u32x2 pk4(f32x4 v) { u32x2 r = {pk2(v[0], v[1]), pk2(v[2], v[3])}; return r; }
DI float bflo(unsigned u) { return __uint_as_float(u << 16); }
DI float bfhi(unsigned u) { return __uint_as_float(u & 0xffff0000u); }
DI float gelu_f(float x) {
  constexpr float C0 = -2.0f * 0.7978845608028654f * 1.4426950408889634f, C1 = C0 * 0.044715f;
  float p = __builtin_fmaf(x * x, C1, C0);
  float e = __builtin_amdgcn_exp2f(p * x);
  return x * __builtin_amdgcn_rcpf(1.0f + e);
}
DI f32x4 gelu4(f32x4 x) {
  constexpr float C0 = -2.0f * 0.7978845608028654f * 1.4426950408889634f, C1 = C0 * 0.044715f;
  f32x4 t = (x * x * C1 + C0) * x, r;
#pragma unroll
  for (int k = 0; k < 4; ++k) r[k] = __builtin_amdgcn_rcpf(1.0f + __builtin_amdgcn_exp2f(t[k]));
  return x * r;
}
template <int CTRL> DI float dpp_add(float x) {
  return x + __builtin_bit_cast(float, __builtin_amdgcn_update_dpp(0, __builtin_bit_cast(int, x), CTRL, 0xf, 0xf, true));
}
DI float red16(float x) {
  x = dpp_add<0xB1>(x); x = dpp_add<0x4E>(x); x = dpp_add<0x141>(x); x = dpp_add<0x140>(x);
  return x;
}
template <int CTRL> DI float dpp_mov(float x) {
  return __builtin_bit_cast(float, __builtin_amdgcn_update_dpp(0, __builtin_bit_cast(int, x), CTRL, 0xf, 0xf, true));
}
DI float red8x16(const float (&v)[8], int c16) {
  const bool b0 = c16 & 1, b1 = c16 & 2, b2 = c16 & 4;
  float a[4], b[2];
#pragma unroll
  for (int k = 0; k < 4; ++k) { float keep = b0 ? v[k + 4] : v[k], give = b0 ? v[k] : v[k + 4]; a[k] = keep + dpp_mov<0xB1>(give); }
#pragma unroll
  for (int k = 0; k < 2; ++k) { float keep = b1 ? a[k + 2] : a[k], give = b1 ? a[k] : a[k + 2]; b[k] = keep + dpp_mov<0x4E>(give); }
  float keep = b2 ? b[1] : b[0], give = b2 ? b[0] : b[1];
  float r = keep + dpp_mov<0x141>(dpp_mov<0x1B>(give));
  r = r + dpp_mov<0x140>(dpp_mov<0x141>(r));
  return r;
}
DI float xq_max(float x) {
  auto a = __builtin_amdgcn_permlane16_swap(__float_as_uint(x), __float_as_uint(x), false, false);
  x = fmaxf(__uint_as_float(a[0]), __uint_as_float(a[1]));
  auto b = __builtin_amdgcn_permlane32_swap(__float_as_uint(x), __float_as_uint(x), false, false);
  return fmaxf(__uint_as_float(b[0]), __uint_as_float(b[1]));
}
DI float xq_sum(float x) {
  auto a = __builtin_amdgcn_permlane16_swap(__float_as_uint(x), __float_as_uint(x), false, false);
  x = __uint_as_float(a[0]) + __uint_as_float(a[1]);
  auto b = __builtin_amdgcn_permlane32_swap(__float_as_uint(x), __float_as_uint(x), false, false);
  return __uint_as_float(b[0]) + __uint_as_float(b[1]);
}

DI void st128(srd_t s, unsigned off, u32x4 v) { __builtin_amdgcn_raw_buffer_store_b128(v, s, off, 0, 16); }
DI void st64(srd_t s, unsigned off, u32x2 v) { __builtin_amdgcn_raw_buffer_store_b64(v, s, off, 0, 16); }
DI u32x4 ld128(srd_t s, unsigned voff, unsigned soff) { return __builtin_amdgcn_raw_buffer_load_b128(s, voff, soff, 16); }

DI void set_flag(char* ws, int idx, u64 tag) {
  __hip_atomic_store((GAS u64*)(ws + WS_FLAGS + (size_t)idx * FLAG_STRIDE), tag, __ATOMIC_RELAXED, __HIP_MEMORY_SCOPE_AGENT);
}
DI void wait_flags(char* ws, int idx, u64 tag) {
  GAS u64* p = (GAS u64*)(ws + WS_FLAGS + (size_t)(idx < 0 ? 0 : idx) * FLAG_STRIDE);
#pragma nounroll
  for (unsigned spin = 0; spin < SPIN_CAP; ++spin) {
    u64 v = __hip_atomic_load(p, __ATOMIC_RELAXED, __HIP_MEMORY_SCOPE_AGENT);
    if (__all(idx < 0 || v == tag)) break;
    __builtin_amdgcn_s_sleep(1);
  }
  asm volatile("" ::: "memory");
}

DI void xmap(int mp, int slot, int T2, int& R, int& c) {
  int m = (mp + T2) & 15, par = (slot >> 3) ^ (m >> 3);
  R = 2 * mp + par; c = (slot & 7) ^ ((m & 7) ^ (par << 1));
}
DI unsigned xaddr(int R, int T, int c) {
  int tau = (R + T) & 31, par = tau & 1, m = tau >> 1;
  return (unsigned)((R >> 1) * 256 + 16 * ((8 * par + c) ^ (8 * (m >> 3) + ((m & 7) ^ (par << 1)))));
}
DI unsigned rec_sum(unsigned a, unsigned b, unsigned thi) { return thi ^ a ^ ((b << 13) | (b >> 19)); }

constexpr int P2D = 8;
struct P2Sched { int wait[32]; int w_poll; int w_epi; };
constexpr P2Sched make_p2(int NF) {
  P2Sched z{};
  int seq[256] = {}; int n = 0;
  for (int s = 0; s < P2D; ++s) for (int f = 0; f < NF; ++f) seq[n++] = s;
  for (int s = 0; s < 32; ++s) {
    int last = -1;
    for (int t = 0; t < n; ++t) if (seq[t] == s) last = t;
    z.wait[s] = n - 1 - last;
    if (s == 32 - P2D) { int lp = -1; for (int t = 0; t < n; ++t) if (seq[t] == 100) lp = t; z.w_poll = n - 1 - lp; }
    if (s + P2D < 32) for (int f = 0; f < NF; ++f) seq[n++] = s + P2D;
    if (s == 8) seq[n++] = 100;
    if (s == 24) { seq[n++] = 101; seq[n++] = 101; }
    if (s == 25) for (int f = 0; f < 2 * NF; ++f) seq[n++] = 101;
    if (s >= 26 && s <= 29) { seq[n++] = 102; seq[n++] = 102; }
    if (s == 30) seq[n++] = 102;
  }
  int last = -1; for (int t = 0; t < n; ++t) if (seq[t] == 101) last = t;
  z.w_epi = n - 1 - last;
  return z;
}
constexpr P2Sched P2S[2] = {make_p2(1), make_p2(2)};

#define STAMP(k) do {} while (0)
#define TS(k) do {} while (0)

__global__ __launch_bounds__(512) __attribute__((amdgpu_waves_per_eu(2, 2)))
void mk_kernel(const __hip_bfloat16* __restrict__ x_, const __hip_bfloat16* __restrict__ wqkv_,
               const __hip_bfloat16* __restrict__ wo_, const __hip_bfloat16* __restrict__ win_,
               const __hip_bfloat16* __restrict__ wsp_, const __hip_bfloat16* __restrict__ bsp_,
               const __hip_bfloat16* __restrict__ wout_, const __hip_bfloat16* __restrict__ gw_,
               __hip_bfloat16* __restrict__ out_, char* __restrict__ ws) {
  extern __shared__ __attribute__((aligned(16))) char smem[];
  const unsigned L0 = (unsigned)(uintptr_t)(LAS char*)smem;
  const int tid = threadIdx.x, lane = tid & 63;
  const int w = (int)rfl((unsigned)tid >> 6);
  const int c16 = lane & 15, q4 = lane >> 4;
  const int tq = (lane & 15) >> 2, tp = lane & 3;
  const int bid = blockIdx.x, xcd = bid & 7, rr = bid >> 3;

  const srd_t sX = __builtin_amdgcn_make_buffer_rsrc((void*)x_, 0, 2048 * 512 * 2, 0x00020000);
  const srd_t sWS = __builtin_amdgcn_make_buffer_rsrc((void*)ws, 0, WS_END, 0x00020000);
  const srd_t sWSP = __builtin_amdgcn_make_buffer_rsrc((void*)wsp_, 0, 2048 * 2048 * 2, 0x00020000);
  u32x2 genv;
  ald64(genv, sWS, WS_GEN, 0);
  STAMP(0);
  TS(0);

  const int p2_q = xcd & 1, p2_i = 8 * (xcd >> 1) + (rr & 7), p2_J = rr >> 3;
  unsigned wsp_voff;
  { int R, c; xmap(4 * w + q4, c16, 0, R, c); wsp_voff = (unsigned)((64 * p2_i + R) * 4096 + p2_q * 2048 + c * 16); }
  auto wsp_issue = [&](int kk) { glds(sWSP, wsp_voff, (unsigned)kk * 128u, L0 + (unsigned)kk * 8192u + (unsigned)w * 1024u); };
  constexpr unsigned LDUMP = 162816;
  constexpr int NWARM = 4;
  const srd_t sWO = __builtin_amdgcn_make_buffer_rsrc((void*)wo_, 0, 512 * 512 * 2, 0x00020000);
  const srd_t sWOUT = __builtin_amdgcn_make_buffer_rsrc((void*)wout_, 0, 688 * 512 * 2, 0x00020000);
  auto warm = [&]() {
    glds4(sWSP, (unsigned)((64 * p2_i + 16 * p2_J + (tid >> 5)) * 4096 + p2_q * 2048 + (tid & 31) * 64), 0, L0 + LDUMP);
    const unsigned g = (unsigned)((rr & 15) * 512 + tid) * 64u;
    if (rr < 16) glds4(sWO, g, 0, L0 + LDUMP); else glds4(sWOUT, g, 0, L0 + LDUMP);
    glds4(sWOUT, (unsigned)(rr * 512 + tid) * 64u + 524288u, 0, L0 + LDUMP);
    glds4(sWS, (unsigned)(lane & 15) * 1048576u + (unsigned)(lane >> 4) * 262144u, 0, L0 + LDUMP);
  };
  u64 tag;
  int late_flag;

  {
    const int i1 = 8 * (xcd & 1) + (rr & 7), cc = rr >> 3, cg = 2 * (xcd >> 1) + (cc & 1);
    if (cc < 2) {
      const int j = cg;
      late_flag = F_Z1 + i1 * 8 + j;
      const srd_t sWIN = __builtin_amdgcn_make_buffer_rsrc((void*)win_, 0, 512 * 1376 * 2, 0x00020000);
      constexpr unsigned SL = 28672, LZ1 = 63488, LSC = 161792;
      const int rp = w & 3, ch = w >> 2;
      unsigned xv0, xv1, wv2a, wv2b, wv1a, wv1b, wv1c;
      { int R, c; xmap(4 * w + q4, c16, 0, R, c); xv0 = (unsigned)((128 * i1 + R) * 1024 + c * 16);
        xmap(4 * (w + 8) + q4, c16, 0, R, c); xv1 = (unsigned)((128 * i1 + R) * 1024 + c * 16); }
      {
        auto wmap = [&](int piece, unsigned& a2, unsigned& a1) {
          int n = 64 * piece + lane, row = n / 12, ccx = n % 12, cb = (ccx >> 1) ^ ((row >> 3) & 1);
          int col = 96 * j + 16 * cb + 8 * (ccx & 1);
          int c2 = 688 + col; if (c2 > 1368) c2 = 1368;
          a1 = (unsigned)(row * 2752 + col * 2); a2 = (unsigned)(row * 2752 + c2 * 2);
        };
        unsigned d;
        wmap(w, wv2a, wv1a); wmap(8 + rp, wv2b, d); wmap(8 + w, d, wv1b); wmap(16 + w, d, wv1c);
        if (ch) wv2b = OOB;
      }
      auto issueS = [&](int s) {
        const unsigned base = L0 + (unsigned)(s % 3) * SL;
        glds(sX, xv0, (unsigned)s * 128u, base + (unsigned)w * 1024u);
        glds(sX, xv1, (unsigned)s * 128u, base + (unsigned)(w + 8) * 1024u);
        glds(sWIN, wv2a, (unsigned)s * (64u * 2752u), base + 16384u + (unsigned)w * 1024u);
        glds(sWIN, wv2b, (unsigned)s * (64u * 2752u), ch ? L0 + LDUMP : base + 16384u + (unsigned)(8 + rp) * 1024u);
      };
      auto issueZ1 = [&]() {
#pragma unroll
        for (int k = 0; k < 4; ++k) {
          const unsigned base = L0 + LZ1 + (unsigned)k * 24576u + (unsigned)w * 1024u;
          glds(sWIN, wv1a, (unsigned)k * (128u * 2752u), base);
          glds(sWIN, wv1b, (unsigned)k * (128u * 2752u), base + 8192u);
          glds(sWIN, wv1c, (unsigned)k * (128u * 2752u), base + 16384u);
        }
      };
      issueS(0); issueS(1);
      warm();
      TS(1);

      unsigned xa[2][2];
#pragma unroll
      for (int b = 0; b < 2; ++b)
#pragma unroll
        for (int ks = 0; ks < 2; ++ks) xa[b][ks] = L0 + xaddr(32 * rp + 2 * c16 + b, 0, 4 * ks + q4);
      unsigned wa[3];
#pragma unroll
      for (int cb = 0; cb < 3; ++cb) wa[cb] = L0 + (unsigned)((8 * q4 + tq) * 192 + 32 * ((3 * ch + cb) ^ (q4 & 1)) + 8 * tp);

      f32x4 az[2][3], a1[3][2];
#pragma unroll
      for (int b = 0; b < 2; ++b)
#pragma unroll
        for (int cb = 0; cb < 3; ++cb) { az[b][cb] = f32x4{0, 0, 0, 0}; a1[cb][b] = f32x4{0, 0, 0, 0}; }
      const int nv = j < 7 ? 3 : (ch == 0 ? 1 : 0);
      bf16x8 xf[16][2];

      sfor<0, 8>([&](auto S_) {
        constexpr int s = decltype(S_)::v;
        wait_bar<(s <= 1 ? 4 + NWARM : (s == 7 ? 0 : 4))>();
        TS(2 + 2 * s);
        constexpr unsigned so = (unsigned)(s % 3) * SL;
        bf16x8 wf[2][3];
#pragma unroll
        for (int ks = 0; ks < 2; ++ks) {
          xf[2 * s + ks][0] = lds128(xa[0][ks] + so); xf[2 * s + ks][1] = lds128(xa[1][ks] + so);
#pragma unroll
          for (int cb = 0; cb < 3; ++cb) wf[ks][cb] = ldstr2(wa[cb] + so + 16384 + ks * 6144, wa[cb] + so + 16384 + ks * 6144 + 768);
        }
        SCHED();
        if constexpr (s + 2 < 8) { if (ch) issueS(s + 2); }
        SCHED();
#pragma unroll
        for (int ks = 0; ks < 2; ++ks)
#pragma unroll
          for (int cb = 0; cb < 3; ++cb) {
            az[0][cb] = mfma32(xf[2 * s + ks][0], wf[ks][cb], az[0][cb]);
            az[1][cb] = mfma32(xf[2 * s + ks][1], wf[ks][cb], az[1][cb]);
          }
        SCHED();
        if constexpr (s + 2 < 8) { if (!ch) issueS(s + 2); }
        TS(3 + 2 * s);
      });
      bar();
      STAMP(1);
      TS(18);
      pin(genv);
      tag = (((u64)genv[1] << 32) | genv[0]) + 1ull;
      const unsigned tlo = (unsigned)tag, thi = (unsigned)(tag >> 32);
      if (ch) issueZ1();
      SCHED();
      {
        float s1[8], s2[8];
#pragma unroll
        for (int b = 0; b < 2; ++b)
#pragma unroll
          for (int cb = 0; cb < 3; ++cb) az[b][cb] = gelu4(az[b][cb]);
        const float en = nv == 3 ? 1.0f : 0.0f;
#pragma unroll
        for (int b = 0; b < 2; ++b) {
          const f32x4 e1 = az[b][1] * en, e2 = az[b][2] * en;
          const f32x4 t1 = az[b][0] + e1 + e2, t2 = az[b][0] * az[b][0] + e1 * e1 + e2 * e2;
#pragma unroll
          for (int r = 0; r < 4; ++r) { s1[2 * r + b] = t1[r]; s2[2 * r + b] = t2[r]; }
        }
        const float sm = red8x16(s1, c16), sv = red8x16(s2, c16);
        const int kidx = 4 * (c16 & 1) + (c16 & 2) + ((c16 >> 2) & 1);
        unsigned so = WS_STATS + (unsigned)(((i1 * 16 + 2 * j + ch) * 128 + 32 * rp + 8 * q4 + kidx) * 16);
        if (c16 >= 8) so = OOB;
        const unsigned a = __float_as_uint(sm), b = __float_as_uint(sv);
        st128(sWS, so, u32x4{a, b, tlo, rec_sum(a, b, thi)});
      }
      SCHED();
      if (!ch) issueZ1();
      TS(19);
      if (j == 7) {
        const unsigned short gv = __builtin_bit_cast(unsigned short, gw_[tid]);
        const int row = tid & 63;
        wait_bar<0>();
        *(LAS unsigned short*)(uintptr_t)(L0 + LZ1 + (unsigned)(tid >> 6) * 12288u + (unsigned)(row * 192 + 32 * (3 ^ ((row >> 3) & 1)))) = gv;
      }
      wait_bar<0>();
      STAMP(2);
      TS(20);
      u32x4 rec[8];
      const unsigned rec_off = WS_STATS + (unsigned)(((i1 * 16 + 8 * (lane >> 5)) * 128 + 32 * rp + (lane & 31)) * 16);
      bf16x8 wf[2][3];
#pragma unroll
      for (int cb = 0; cb < 3; ++cb) wf[0][cb] = ldstr2(wa[cb] + LZ1, wa[cb] + LZ1 + 768);
      sfor<0, 16>([&](auto U_) {
        constexpr int u = decltype(U_)::v;
        if constexpr (u == 8) { TS(21); bar(); TS(22); }
        if constexpr (u + 1 < 16) {
          constexpr unsigned wo = LZ1 + (unsigned)(u + 1) * 6144u;
#pragma unroll
          for (int cb = 0; cb < 3; ++cb) wf[(u + 1) & 1][cb] = ldstr2(wa[cb] + wo, wa[cb] + wo + 768);
        }
#pragma unroll
        for (int cb = 0; cb < 3; ++cb) {
          a1[cb][0] = mfma32(wf[u & 1][cb], xf[u][0], a1[cb][0]);
          a1[cb][1] = mfma32(wf[u & 1][cb], xf[u][1], a1[cb][1]);
        }
        SCHED();
        if constexpr (u < 4) { wsp_issue(u); }
        if constexpr (u >= 4 && u < 8) { ald128(rec[2 * (u - 4)], sWS, rec_off, (unsigned)(2 * (u - 4)) * 2048u); ald128(rec[2 * (u - 4) + 1], sWS, rec_off, (unsigned)(2 * (u - 4) + 1) * 2048u); }
        if constexpr (u >= 10 && u < 13) { wsp_issue(4 + 2 * (u - 10)); wsp_issue(5 + 2 * (u - 10)); }
        if constexpr (u >= 13) { wsp_issue(u - 3); }
        SCHED();
        if constexpr (u == 9) {
          TS(23);
          wait_vm<0>();
          TS(24);
#pragma unroll
          for (int k = 0; k < 8; ++k) pin(rec[k]);
#pragma nounroll
          for (unsigned spin = 0; spin < SPIN_CAP; ++spin) {
            bool ok = true;
#pragma unroll
            for (int k = 0; k < 8; ++k) ok &= (rec[k][2] == tlo && rec[k][3] == rec_sum(rec[k][0], rec[k][1], thi));
            if (__all(ok)) break;
            __builtin_amdgcn_s_sleep(1);
#pragma unroll
            for (int k = 0; k < 8; ++k) rec[k] = ld128(sWS, rec_off, (unsigned)k * 2048u);
          }
          TS(25);
          if (lane >= 32) { rec[7][0] = 0u; rec[7][1] = 0u; }
          float t1 = 0.f, t2 = 0.f;
#pragma unroll
          for (int k = 0; k < 8; ++k) { t1 += __uint_as_float(rec[k][0]); t2 += __uint_as_float(rec[k][1]); }
          { auto e = __builtin_amdgcn_permlane32_swap(__float_as_uint(t1), __float_as_uint(t1), false, false); t1 = __uint_as_float(e[0]) + __uint_as_float(e[1]); }
          { auto e = __builtin_amdgcn_permlane32_swap(__float_as_uint(t2), __float_as_uint(t2), false, false); t2 = __uint_as_float(e[0]) + __uint_as_float(e[1]); }
          const float mean = t1 * (1.0f / 688.0f);
          const float var = fmaxf(t2 * (1.0f / 688.0f) - mean * mean, 0.f);
          const float rstd = __builtin_amdgcn_rsqf(var + 1e-5f);
          const unsigned sc = L0 + LSC + (unsigned)w * 256u;
          if (lane < 32) lds_st64(sc + (unsigned)lane * 8u, u32x2{__float_as_uint(rstd), __float_as_uint(-mean * rstd)});
          asm volatile("s_waitcnt lgkmcnt(0)" ::: "memory");
          f32x4 st[4];
#pragma unroll
          for (int r = 0; r < 4; ++r) st[r] = lds128f(sc + (unsigned)(8 * q4 + 2 * r) * 8u);
#pragma unroll
          for (int cb = 0; cb < 3; ++cb) {
            u32x4 o;
#pragma unroll
            for (int r = 0; r < 4; ++r) o[r] = pk2(__builtin_fmaf(az[0][cb][r], st[r][0], st[r][1]), __builtin_fmaf(az[1][cb][r], st[r][2], st[r][3]));
            unsigned zo = WS_Z2N + (unsigned)((((4 * i1 + rp) * 44 + 6 * j + 3 * ch + cb) * 64 + lane) * 16);
            if (cb >= nv) zo = OOB;
            st128(sWS, zo, o);
          }
          SCHED();
          TS(26);
        }
      });
      TS(27);
      wait_bar<9>();
      TS(28);
      if (tid == 0) set_flag(ws, F_Z2 + i1 * 8 + j, tag);
      if (ch) { wsp_issue(13); wsp_issue(14); wsp_issue(15); }
      SCHED();
      STAMP(3);
      TS(29);
      {
        float g0 = __builtin_amdgcn_rcpf(1.0f + __builtin_amdgcn_exp2f(-1.4426950408889634f * a1[0][0][0]));
        float g1 = __builtin_amdgcn_rcpf(1.0f + __builtin_amdgcn_exp2f(-1.4426950408889634f * a1[0][1][0]));
        unsigned go = WS_GATE + (unsigned)((128 * i1 + 32 * rp + 2 * c16) * 4);
        if (q4 != 0 || nv != 0) go = OOB;
        st64(sWS, go, u32x2{__float_as_uint(g0), __float_as_uint(g1)});
#pragma unroll
        for (int cb = 0; cb < 3; ++cb) {
          const u32x2 p0 = pk4(gelu4(a1[cb][0])), p1 = pk4(gelu4(a1[cb][1]));
          unsigned zo = WS_Z1 + (unsigned)((((4 * i1 + rp) * 44 + 6 * j + 3 * ch + cb) * 64 + lane) * 16);
          if (cb >= nv) zo = OOB;
          st128(sWS, zo, u32x4{p0[0], p0[1], p1[0], p1[1]});
        }
      }
      SCHED();
      if (!ch) { wsp_issue(13); wsp_issue(14); wsp_issue(15); }
    } else {
      const int hg = cg;
      late_flag = F_O + i1 * 8 + hg;
      const srd_t sWQ = __builtin_amdgcn_make_buffer_rsrc((void*)wqkv_, 0, 512 * 1536 * 2, 0x00020000);
      constexpr unsigned SLOT = 43008, LWQ = 18432, LSC = 131072;
      const int h = w & 3, th = w >> 2;
      unsigned xv0, xv1, xvh, wv;
      { int R, c, t;
        xmap(4 * (2 + w) + q4, c16, 8, R, c); xv0 = (unsigned)((128 * i1 - 16 + R) * 1024 + c * 16);
        xmap(4 * (10 + w) + q4, c16, 8, R, c); xv1 = (unsigned)((128 * i1 - 16 + R) * 1024 + c * 16);
        xmap(w, lane >> 2, 8, R, c); t = 128 * i1 - 16 + R; if (t < 0) t = 0; xvh = (unsigned)(t * 1024 + c * 16 + (lane & 3) * 4); }
      { int row = 8 * w + (lane >> 3), cx = lane & 7, f = ((row >> 1) & 1) | ((row >> 2) & 2);
        int col = 64 * hg + 16 * ((cx >> 1) ^ f) + 8 * (cx & 1);
        wv = (unsigned)(row * 3072 + col * 2); }
      auto issue = [&](int s) {
        const unsigned base = L0 + (unsigned)(s % 3) * SLOT;
        glds(sX, xv0, (unsigned)s * 128u, base + (unsigned)(2 + w) * 1024u);
        glds(sX, xv1, (unsigned)s * 128u, base + (unsigned)(10 + w) * 1024u);
        glds4(sX, xvh, (unsigned)s * 128u, base + (unsigned)w * 256u);
#pragma unroll
        for (int sg = 0; sg < 3; ++sg) glds(sWQ, wv, (unsigned)s * (64u * 3072u) + (unsigned)sg * 1024u, base + LWQ + (unsigned)sg * 8192u + (unsigned)w * 1024u);
      };
      issue(0); issue(1);
      warm();
      TS(1);
      unsigned xa[2][2], xh[2];
#pragma unroll
      for (int ks = 0; ks < 2; ++ks) {
#pragma unroll
        for (int par = 0; par < 2; ++par) xa[par][ks] = L0 + xaddr(16 + 64 * th + 2 * c16 + par, 16, 4 * ks + q4);
        xh[ks] = L0 + xaddr(c16, 16, 4 * ks + q4);
      }
      const unsigned wa = L0 + LWQ + (unsigned)((8 * q4 + tq) * 128 + 32 * (h ^ ((tq >> 1) | ((q4 & 1) << 1))) + 8 * tp);
      f32x4 aq[4], ak[5], av[5];
#pragma unroll
      for (int n = 0; n < 4; ++n) aq[n] = f32x4{0, 0, 0, 0};
#pragma unroll
      for (int n = 0; n < 5; ++n) { ak[n] = f32x4{0, 0, 0, 0}; av[n] = f32x4{0, 0, 0, 0}; }
      auto qloop = [&](auto HALO_) {
        constexpr bool HALO = decltype(HALO_)::v != 0;
        sfor<0, 8>([&](auto S_) {
          constexpr int s = decltype(S_)::v;
          wait_bar<(s <= 1 ? 6 + NWARM : (s == 7 ? 5 : 6))>();
          TS(2 + 2 * s);
          auto dma = [&]() {
            if constexpr (s + 2 < 8) issue(s + 2);
            if constexpr (s == 6) { wsp_issue(11); wsp_issue(12); wsp_issue(13); wsp_issue(14); wsp_issue(15); }
          };
          constexpr unsigned so = (unsigned)(s % 3) * SLOT;
          bf16x8 wq[2], wk[2], wvv[2], xf[2][5];
#pragma unroll
          for (int ks = 0; ks < 2; ++ks) {
            wq[ks] = ldstr2(wa + so + ks * 4096, wa + so + ks * 4096 + 512);
            wk[ks] = ldstr2(wa + so + 8192 + ks * 4096, wa + so + 8192 + ks * 4096 + 512);
            wvv[ks] = ldstr2(wa + so + 16384 + ks * 4096, wa + so + 16384 + ks * 4096 + 512);
#pragma unroll
            for (int n = 0; n < 4; ++n) xf[ks][n] = lds128(xa[n & 1][ks] + so + (unsigned)(n >> 1) * 4096u);
            if constexpr (HALO) xf[ks][4] = lds128(xh[ks] + so);
          }
          SCHED();
          if constexpr (!HALO) dma();
          SCHED();
#pragma unroll
          for (int ks = 0; ks < 2; ++ks) {
#pragma unroll
            for (int n = 0; n < 4; ++n) {
              aq[n] = mfma32(wq[ks], xf[ks][n], aq[n]);
              ak[n] = mfma32(wk[ks], xf[ks][n], ak[n]);
              av[n] = mfma32(xf[ks][n], wvv[ks], av[n]);
            }
            if constexpr (HALO) {
              ak[4] = mfma32(wk[ks], xf[ks][4], ak[4]);
              av[4] = mfma32(xf[ks][4], wvv[ks], av[4]);
            }
          }
          SCHED();
          if constexpr (HALO) dma();
          TS(3 + 2 * s);
        });
      };
      if (th == 0) qloop(IC<1>{}); else qloop(IC<0>{});
      STAMP(1);
      TS(18);
      pin(genv);
      tag = (((u64)genv[1] << 32) | genv[0]) + 1ull;
      u32x2 qb[4], kb[5], vb[5];
#pragma unroll
      for (int n = 0; n < 4; ++n) qb[n] = pk4(aq[n]);
#pragma unroll
      for (int n = 0; n < 5; ++n) { kb[n] = pk4(ak[n]); vb[n] = pk4(av[n]); }
      const unsigned ex = L0 + LSC + (unsigned)h * 2048u + (unsigned)lane * 8u;
      if (th == 0) { lds_st64(ex, kb[2]); lds_st64(ex + 512, kb[3]); lds_st64(ex + 1024, vb[2]); lds_st64(ex + 1536, vb[3]); }
      bar();
      if (th) {
#pragma unroll
        for (int kk = 0; kk < 11; ++kk) wsp_issue(kk);
      }
      SCHED();
      TS(20);
      u32x2 kp[2], vp[2];
      if (th == 0) { kp[0] = kb[4]; kp[1] = kb[4]; vp[0] = vb[4]; vp[1] = vb[4]; }
      else { kp[0] = lds64(ex); kp[1] = lds64(ex + 512); vp[0] = lds64(ex + 1024); vp[1] = lds64(ex + 1536); }
      constexpr float NEG = -1e30f;
      f32x4 bias_c, bias_p, bias_f[2];
      unsigned mc[2], mp_[2], mf[2][2];
      {
        unsigned vc = 0, vpv = 0, vh0 = 0, vh1 = 0;
#pragma unroll
        for (int r = 0; r < 4; ++r) {
          const int kap = 4 * q4 + r;
          const bool c_ok = (kap <= c16) && (kap >= c16 - 7);
          const bool p_ok = kap >= c16 + 9;
          const bool h0 = th ? p_ok : ((i1 > 0) && ((kap & 1) == 0) && (kap >= 2 * c16 + 2));
          const bool h1 = th ? p_ok : ((i1 > 0) && ((kap & 1) == 1) && (kap >= 2 * c16 + 3));
          bias_c[r] = c_ok ? 0.f : NEG; bias_p[r] = p_ok ? 0.f : NEG; bias_f[0][r] = h0 ? 0.f : NEG; bias_f[1][r] = h1 ? 0.f : NEG;
          vc |= (unsigned)c_ok << r; vpv |= (unsigned)p_ok << r; vh0 |= (unsigned)h0 << r; vh1 |= (unsigned)h1 << r;
        }
        auto mk = [](unsigned v, int k) { return ((v >> (2 * k)) & 1u ? 0xffffu : 0u) | ((v >> (2 * k + 1)) & 1u ? 0xffff0000u : 0u); };
#pragma unroll
        for (int k = 0; k < 2; ++k) { mc[k] = mk(vc, k); mp_[k] = mk(vpv, k); mf[0][k] = mk(vh0, k); mf[1][k] = mk(vh1, k); }
      }
      TS(21);
      constexpr float C2 = 0.25f * 1.4426950408889634f;
#pragma unroll
      for (int n = 0; n < 4; ++n) {
        const bool first = n < 2;
        const u32x2 kprev = first ? kp[n & 1] : kb[n - 2], vprev = first ? vp[n & 1] : vb[n - 2];
        const f32x4 bp = first ? bias_f[n & 1] : bias_p;
        const unsigned mp0 = first ? mf[n & 1][0] : mp_[0], mp1 = first ? mf[n & 1][1] : mp_[1];
        f32x4 sp = mfma16(kprev, qb[n], bp), sc = mfma16(kb[n], qb[n], bias_c);
        f32x4 sm;
#pragma unroll
        for (int r = 0; r < 4; ++r) sm[r] = fmaxf(sp[r], sc[r]);
        float m = xq_max(fmaxf(fmaxf(sm[0], sm[1]), fmaxf(sm[2], sm[3])));
        f32x4 p;
#pragma unroll
        for (int r = 0; r < 4; ++r) p[r] = __builtin_amdgcn_exp2f((sm[r] - m) * C2);
        const float inv = __builtin_amdgcn_rcpf(xq_sum((p[0] + p[1]) + (p[2] + p[3])));
#pragma unroll
        for (int r = 0; r < 4; ++r) p[r] *= inv;
        const u32x2 pp = pk4(p);
        u32x4 pb = {pp[0] & mp0, pp[1] & mp1, pp[0] & mc[0], pp[1] & mc[1]};
        u32x4 va = {vprev[0], vprev[1], vb[n][0], vb[n][1]};
        f32x4 o = mfma32(asbf(va), asbf(pb), f32x4{0, 0, 0, 0});
        const int tbl = 4 * th + n;
        lds_st64(L0 + LSC + 8192 + (unsigned)(((tbl * 2 + (h >> 1)) * 64 + lane) * 16 + (h & 1) * 8), pk4(o));
      }
      SCHED();
      if (!th) {
#pragma unroll
        for (int kk = 0; kk < 11; ++kk) wsp_issue(kk);
      }
      TS(27);
      bar();
      TS(28);
#pragma unroll
      for (int k = 0; k < 2; ++k) {
        const int f = w + 8 * k;
        u32x4 v = lds128u(L0 + LSC + 8192 + (unsigned)(f * 1024 + lane * 16));
        st128(sWS, WS_O + (unsigned)((((8 * i1 + (f >> 1)) * 16 + 2 * hg + (f & 1)) * 64 + lane) * 16), v);
      }
      STAMP(3);
    }
  }

  const int i3 = 4 * xcd + (rr & 3), j3 = rr >> 2;
  const int t2 = w & 1, kq = w >> 1;
  u32x4 of[4][2];
  u32x2 gg;
  {
    const int q = p2_q, i2 = p2_i, J = p2_J;
    const srd_t sBSP = __builtin_amdgcn_make_buffer_rsrc((void*)bsp_, 0, 2048 * 2, 0x00020000);
    const int fb = w < 4 ? 12 * J + 2 * w : 12 * J + 4 + w;
    const int nf = w < 4 ? (fb + 1 <= 42 ? 2 : 1) : (fb <= 42 ? 1 : 0);
    const int lf_idx = lane < 2 ? F_Z1 + (i2 >> 1) * 8 + 2 * J + lane
                                : (lane < 10 ? F_O + (i3 >> 1) * 8 + (lane - 2) : (lane == 10 ? F_Z1 + (i3 >> 1) * 8 + 7 : -1));
    TS(30);
    wait_flags(ws, lane < 16 ? F_Z2 + (8 * q + (lane >> 1)) * 8 + 2 * J + (lane & 1) : -1, tag);
    STAMP(4);
    TS(31);
    unsigned ta[2][2];
#pragma unroll
    for (int par = 0; par < 2; ++par)
#pragma unroll
      for (int ks = 0; ks < 2; ++ks) ta[par][ks] = L0 + xaddr(2 * c16 + par, 0, 4 * ks + q4);
    const unsigned zv = WS_Z2N + (unsigned)(((32 * q) * 44 + fb) * 1024 + lane * 16);
    const unsigned ov = WS_O + (unsigned)((((4 * i3 + 2 * t2) * 16 + 4 * kq) * 64 + lane) * 16);
    const unsigned gv = WS_GATE + (unsigned)((64 * i3 + 32 * t2 + 2 * c16) * 4);
    const unsigned z1v = WS_Z1 + (unsigned)((((2 * i2) * 44 + fb) * 64 + lane) * 16);
    f32x4 acc[2][4];
#pragma unroll
    for (int f = 0; f < 2; ++f)
#pragma unroll
      for (int t = 0; t < 4; ++t) acc[f][t] = f32x4{0, 0, 0, 0};
    unsigned bb[2];
    u32x4 z1f[2][2];
#pragma unroll
    for (int f = 0; f < 2; ++f) { z1f[f][0] = u32x4{0, 0, 0, 0}; z1f[f][1] = u32x4{0, 0, 0, 0}; }
    auto body = [&](auto NF_) {
      constexpr int NF = decltype(NF_)::v;
      constexpr int D = P2D;
      constexpr P2Sched SC = P2S[NF - 1];
      u32x4 zb[D][NF];
      u32x2 lfv;
      sfor<0, D>([&](auto S_) { constexpr int s = decltype(S_)::v;
#pragma unroll
        for (int f = 0; f < NF; ++f) ald128(zb[s][f], sWS, zv + f * 1024, (unsigned)s * 45056u); });
      TS(32);
      wait_bar<D * NF>();
      TS(33);
      if (tid == 0) set_flag(ws, late_flag, tag);
      bf16x8 tf[2][4];
#pragma unroll
      for (int t = 0; t < 4; ++t) tf[0][t] = lds128(ta[t & 1][0] + (unsigned)(t >> 1) * 4096u);
      sfor<0, 32>([&](auto S_) {
        constexpr int s = decltype(S_)::v;
        if constexpr (s + 1 < 32) {
          constexpr unsigned ko = (unsigned)((s + 1) >> 1) * 8192u;
#pragma unroll
          for (int t = 0; t < 4; ++t) tf[(s + 1) & 1][t] = lds128(ta[t & 1][(s + 1) & 1] + ko + (unsigned)(t >> 1) * 4096u);
        }
        if constexpr ((s & 3) == 0) TS(34 + (s >> 2));
        wait_vm<SC.wait[s]>();
#pragma unroll
        for (int f = 0; f < NF; ++f) pin(zb[s % D][f]);
#pragma unroll
        for (int f = 0; f < NF; ++f)
#pragma unroll
          for (int t = 0; t < 4; ++t) acc[f][t] = mfma32(asbf(zb[s % D][f]), tf[s & 1][t], acc[f][t]);
        SCHED();
        if constexpr (s + D < 32) {
#pragma unroll
          for (int f = 0; f < NF; ++f) ald128(zb[s % D][f], sWS, zv + f * 1024, (unsigned)(s + D) * 45056u);
        }
        if constexpr (s == 8) ald64(lfv, sWS, lf_idx < 0 ? WS_FLAGS : WS_FLAGS + (unsigned)lf_idx * FLAG_STRIDE, 0);
        if constexpr (s == 24) {
          static_assert(SC.w_poll >= SC.wait[24], "poll must be retired");
          pin(lfv);
          if (!__all(lf_idx < 0 || ((((u64)lfv[1] << 32) | lfv[0]) == tag))) wait_flags(ws, lf_idx, tag);
          ald32(bb[0], sBSP, (unsigned)((64 * i2 + 2 * c16) * 2), 0);
          ald32(bb[1], sBSP, (unsigned)((64 * i2 + 32 + 2 * c16) * 2), 0);
        }
        if constexpr (s == 25) {
#pragma unroll
          for (int f = 0; f < NF; ++f) { ald128(z1f[f][0], sWS, z1v + f * 1024, 0); ald128(z1f[f][1], sWS, z1v + f * 1024, 45056u); }
        }
        if constexpr (s >= 26 && s <= 29) { ald128(of[s - 26][0], sWS, ov + (s - 26) * 1024, 0); ald128(of[s - 26][1], sWS, ov + (s - 26) * 1024, 16384u); }
        if constexpr (s == 30) ald64(gg, sWS, gv, 0);
        SCHED();
      });
      TS(42);
      wait_vm<SC.w_epi>();
      pin(bb[0]); pin(bb[1]);
#pragma unroll
      for (int f = 0; f < NF; ++f) { pin(z1f[f][0]); pin(z1f[f][1]); }
    };
    if (nf == 2) body(IC<2>{});
    else if (nf == 1) body(IC<1>{});
    else {
      wait_bar<0>();
      if (tid == 0) set_flag(ws, late_flag, tag);
      wait_flags(ws, lf_idx, tag);
      bb[0] = 0; bb[1] = 0;
#pragma unroll
      for (int k = 0; k < 4; ++k) { ald128(of[k][0], sWS, ov + k * 1024, 0); ald128(of[k][1], sWS, ov + k * 1024, 16384u); }
      ald64(gg, sWS, gv, 0);
    }
    STAMP(5);
    TS(43);
    {
      float bs[4];
#pragma unroll
      for (int g2 = 0; g2 < 2; ++g2) { bs[2 * g2] = q ? 0.f : bflo(bb[g2]); bs[2 * g2 + 1] = q ? 0.f : bfhi(bb[g2]); }
      u32x2 ub[2][4];
#pragma unroll
      for (int f = 0; f < 2; ++f)
#pragma unroll
        for (int t = 0; t < 4; ++t) {
          const u32x4 z = z1f[f][t >> 1];
          const int par = t & 1;
          f32x4 v;
          v[0] = bflo(z[2 * par]) * (acc[f][t][0] + bs[t]); v[1] = bfhi(z[2 * par]) * (acc[f][t][1] + bs[t]);
          v[2] = bflo(z[2 * par + 1]) * (acc[f][t][2] + bs[t]); v[3] = bfhi(z[2 * par + 1]) * (acc[f][t][3] + bs[t]);
          ub[f][t] = pk4(v);
        }
      const unsigned uo = WS_U + (unsigned)((((q * 128 + 4 * i2) * 22 + (fb >> 1)) * 64 + lane) * 16);
      if (w < 4) {
#pragma unroll
        for (int t = 0; t < 4; ++t) st128(sWS, uo + (unsigned)t * 22528u, u32x4{ub[0][t][0], ub[0][t][1], ub[1][t][0], ub[1][t][1]});
      } else {
#pragma unroll
        for (int t = 0; t < 4; ++t) st64(sWS, nf ? uo + (unsigned)t * 22528u + (unsigned)(fb & 1) * 8u : OOB, ub[0][t]);
      }
    }
    TS(44);
    bar();
    TS(45);
    const int brow = 8 * w + (lane >> 3), bcx = lane & 7;
    const unsigned bv = (unsigned)(brow * 1024 + (64 * j3 + 16 * ((bcx >> 1) ^ ((brow >> 1) & 3)) + 8 * (bcx & 1)) * 2);
#pragma unroll
    for (int n = 0; n < 4; ++n) glds(sWO, bv, (unsigned)n * 65536u, L0 + (unsigned)(n * 8 + w) * 1024u);
    TS(46);
    wait_bar<4>();
    TS(47);
    if (tid == 0) set_flag(ws, F_U + (i2 * 4 + J) * 2 + q, tag);
#pragma unroll
    for (int n = 4; n < 8; ++n) glds(sWO, bv, (unsigned)n * 65536u, L0 + (unsigned)(n * 8 + w) * 1024u);
#pragma unroll
    for (int n = 0; n < 11; ++n) glds(sWOUT, bv, (unsigned)n * 65536u, L0 + 65536u + (unsigned)(n * 8 + w) * 1024u);
    STAMP(6);
  }

  {
    wait_flags(ws, lane < 8 ? F_U + i3 * 8 + lane : -1, tag);
    STAMP(7);
    TS(48);
    const int p0 = kq < 2 ? 6 * kq : 12 + 5 * (kq - 2), np = kq < 2 ? 6 : 5;
    u32x4 uf[6][2][2];
    const unsigned uv = WS_U + (unsigned)((((4 * i3 + 2 * t2) * 22 + p0) * 64 + lane) * 16);
    auto uld = [&](auto K_) {
      constexpr int k = decltype(K_)::v;
#pragma unroll
      for (int qq = 0; qq < 2; ++qq)
#pragma unroll
        for (int t = 0; t < 2; ++t)
          ald128(uf[k][qq][t], sWS, (k == 5 && np == 5) ? OOB : uv + (unsigned)k * 1024u, (unsigned)(qq * 128 + t) * 22528u);
    };
    uld(IC<0>{});
    TS(49);
    wait_bar<4>();
#pragma unroll
    for (int k = 0; k < 4; ++k) { pin(of[k][0]); pin(of[k][1]); }
    pin(gg);
    TS(50);
    const unsigned wb = (unsigned)((4 * q4 + tq) * 128 + 32 * (2 * (q4 & 1) + (tq >> 1)) + 8 * tp);
    f32x4 aa[4][2], ab[4][2];
#pragma unroll
    for (int cb = 0; cb < 4; ++cb)
#pragma unroll
      for (int t = 0; t < 2; ++t) { aa[cb][t] = f32x4{0, 0, 0, 0}; ab[cb][t] = f32x4{0, 0, 0, 0}; }
    const unsigned ba = L0 + (unsigned)(4 * kq) * 4096u, bbs = L0 + 65536u + (unsigned)p0 * 4096u;
    sfor<0, 4>([&](auto K_) {
      constexpr int k = decltype(K_)::v;
      bf16x8 wf[4];
#pragma unroll
      for (int cb = 0; cb < 4; ++cb) {
        const unsigned a = (wb ^ (unsigned)(cb << 5)) + ba + (unsigned)k * 4096u;
        wf[cb] = ldstr2(a, a + 2048);
      }
      SCHED();
      uld(IC<k + 1>{});
      SCHED();
#pragma unroll
      for (int cb = 0; cb < 4; ++cb)
#pragma unroll
        for (int t = 0; t < 2; ++t) aa[cb][t] = mfma32(wf[cb], asbf(of[k][t]), aa[cb][t]);
      SCHED();
    });
    uld(IC<5>{});
    STAMP(8);
    TS(51);
    sfor<0, 6>([&](auto K_) {
      constexpr int k = decltype(K_)::v;
      bf16x8 wf[4];
#pragma unroll
      for (int cb = 0; cb < 4; ++cb) {
        const unsigned a = (wb ^ (unsigned)(cb << 5)) + bbs + ((k == 5 && np == 5) ? 0u : (unsigned)k * 4096u);
        wf[cb] = ldstr2(a, a + 2048);
      }
      wait_vm<4 * (5 - k)>();
#pragma unroll
      for (int qq = 0; qq < 2; ++qq)
#pragma unroll
        for (int t = 0; t < 2; ++t) pin(uf[k][qq][t]);
#pragma unroll
      for (int cb = 0; cb < 4; ++cb)
#pragma unroll
        for (int qq = 0; qq < 2; ++qq)
#pragma unroll
          for (int t = 0; t < 2; ++t) ab[cb][t] = mfma32(wf[cb], asbf(uf[k][qq][t]), ab[cb][t]);
      SCHED();
    });
    if (bid == 0 && w == 7) {
#pragma unroll
      for (int k = 0; k < 4; ++k) wait_flags(ws, F_Z1 + 64 * k + lane, tag);
      if (lane == 0) __hip_atomic_store((GAS u64*)(ws + WS_GEN), tag, __ATOMIC_RELAXED, __HIP_MEMORY_SCOPE_AGENT);
    }
    TS(52);
    bar();
    TS(53);
    constexpr unsigned RP = 272;
#pragma unroll
    for (int t = 0; t < 2; ++t) {
      const float g = __uint_as_float(gg[t]), g1 = 1.0f - g;
      const unsigned ro = L0 + (unsigned)(kq * 64 + 32 * t2 + 2 * c16 + t) * RP + (unsigned)q4 * 16u;
#pragma unroll
      for (int cb = 0; cb < 4; ++cb) {
        f32x4 v;
#pragma unroll
        for (int r = 0; r < 4; ++r) v[r] = g * aa[cb][t][r] + g1 * ab[cb][t][r];
        lds_st128(ro + (unsigned)cb * 64u, v);
      }
    }
    TS(54);
    bar();
    TS(55);
    {
      const int tok = tid >> 3, chn = tid & 7;
      f32x4 s0 = f32x4{0, 0, 0, 0}, s1 = f32x4{0, 0, 0, 0};
#pragma unroll
      for (int k = 0; k < 4; ++k) {
        const unsigned ro = L0 + (unsigned)(k * 64 + tok) * RP + (unsigned)chn * 32u;
        s0 += lds128f(ro); s1 += lds128f(ro + 16);
      }
      u32x2 a = pk4(s0), b = pk4(s1);
      *(u32x4*)(out_ + (size_t)(64 * i3 + tok) * 512 + 64 * j3 + 8 * chn) = u32x4{a[0], a[1], b[0], b[1]};
    }
    STAMP(9);
    TS(56);
  }
}

extern "C" void kernel_launch(void* const* d_in, const int* in_sizes, int n_in, void* d_out, int out_size, void* d_ws, size_t ws_size,
                              hipStream_t stream) {
  const __hip_bfloat16* x = (const __hip_bfloat16*)d_in[0];
  const __hip_bfloat16* wqkv = (const __hip_bfloat16*)d_in[1];
  const __hip_bfloat16* wo = (const __hip_bfloat16*)d_in[2];
  const __hip_bfloat16* win = (const __hip_bfloat16*)d_in[4];
  const __hip_bfloat16* wsp = (const __hip_bfloat16*)d_in[5];
  const __hip_bfloat16* bsp = (const __hip_bfloat16*)d_in[6];
  const __hip_bfloat16* wout = (const __hip_bfloat16*)d_in[7];
  const __hip_bfloat16* gw = (const __hip_bfloat16*)d_in[8];
  (void)hipFuncSetAttribute((const void*)mk_kernel, hipFuncAttributeMaxDynamicSharedMemorySize, LDS_BYTES);
  mk_kernel<<<256, 512, LDS_BYTES, stream>>>(x, wqkv, wo, win, wsp, bsp, wout, gw, (__hip_bfloat16*)d_out, (char*)d_ws);
}
```

```cpp
#include <hip/hip_runtime.h>
#include <hip/hip_bf16.h>
#include <stdint.h>

#define DI __device__ __forceinline__
#define GAS __attribute__((address_space(1)))
#define LAS __attribute__((address_space(3)))
typedef __attribute__((ext_vector_type(8))) __bf16 bf16x8;
typedef __attribute__((ext_vector_type(2))) __bf16 bf16x2;
typedef __attribute__((ext_vector_type(4))) short s16x4;
typedef __attribute__((ext_vector_type(4))) float f32x4;
typedef __attribute__((ext_vector_type(2))) float f32x2;
typedef __attribute__((ext_vector_type(4))) unsigned u32x4;
typedef __attribute__((ext_vector_type(2))) unsigned u32x2;
typedef unsigned long long u64;
typedef __amdgpu_buffer_rsrc_t srd_t;

constexpr unsigned WS_GEN = 0, WS_FLAGS = 4096, FLAG_STRIDE = 128;
constexpr int F_Z2 = 128, F_Z1 = 256, F_O = 384, F_U = 512;
constexpr unsigned WS_GATE = 131072, WS_Z2N = 524288, WS_Z1 = 3407872, WS_O = 6291456, WS_U = 8388608, WS_STATS = 14155776;
constexpr unsigned WS_END = WS_STATS + 524288;
constexpr int LDS_BYTES = 163840;
constexpr unsigned SPIN_CAP = 1u << 16;
constexpr unsigned OOB = 0x7fff0000u;

template <int N> struct IC { static constexpr int v = N; };
template <int B, int E, class F> DI void sfor(F&& f) { if constexpr (B < E) { f(IC<B>{}); sfor<B + 1, E>(f); } }
#define SCHED() __builtin_amdgcn_sched_barrier(0)
#define UDEPTH 2
#define P2R 4
#define UROT 1
#define ARRU_ 13
#define RECU_ 7

DI unsigned rfl(unsigned x) { return __builtin_amdgcn_readfirstlane(x); }
DI void glds(srd_t srd, unsigned voff, unsigned soff, unsigned ldsdst) {
  asm volatile("s_mov_b32 m0, %0\n\ts_nop 3\n\tbuffer_load_dwordx4 %1, %2, %3 offen lds"
               :: "s"(ldsdst), "v"(voff), "s"(srd), "s"(soff) : "m0", "memory");
}
DI void glds4(srd_t srd, unsigned voff, unsigned soff, unsigned ldsdst) {
  asm volatile("s_mov_b32 m0, %0\n\ts_nop 3\n\tbuffer_load_dword %1, %2, %3 offen lds"
               :: "s"(ldsdst), "v"(voff), "s"(srd), "s"(soff) : "m0", "memory");
}
template <int N> DI void wait_vm() { asm volatile("s_waitcnt vmcnt(%0)" :: "n"(N) : "memory"); }
template <int N> DI void wait_bar() { asm volatile("s_waitcnt vmcnt(%0) lgkmcnt(0)\n\ts_barrier" :: "n"(N) : "memory"); }
DI void bar() { asm volatile("s_waitcnt lgkmcnt(0)\n\ts_barrier" ::: "memory"); }
DI void ald128(u32x4& d, srd_t s, unsigned voff, unsigned soff) {
  asm volatile("s_nop 4\n\tbuffer_load_dwordx4 %0, %1, %2, %3 offen sc1" : "=v"(d) : "v"(voff), "s"(s), "s"(soff) : "memory");
}
DI void ald64(u32x2& d, srd_t s, unsigned voff, unsigned soff) {
  asm volatile("s_nop 4\n\tbuffer_load_dwordx2 %0, %1, %2, %3 offen sc1" : "=v"(d) : "v"(voff), "s"(s), "s"(soff) : "memory");
}
DI void ald32(unsigned& d, srd_t s, unsigned voff, unsigned soff) {
  asm volatile("s_nop 4\n\tbuffer_load_dword %0, %1, %2, %3 offen" : "=v"(d) : "v"(voff), "s"(s), "s"(soff) : "memory");
}
template <class T> DI void pin(T& x) { asm volatile("" : "+v"(x)); }

DI bf16x8 lds128(unsigned a) { return *(const LAS bf16x8*)(uintptr_t)a; }
DI f32x4 lds128f(unsigned a) { return *(const LAS f32x4*)(uintptr_t)a; }
DI void lds_st128(unsigned a, f32x4 v) { *(LAS f32x4*)(uintptr_t)a = v; }
DI void lds_st64(unsigned a, u32x2 v) { *(LAS u32x2*)(uintptr_t)a = v; }
DI u32x2 lds64(unsigned a) { return *(const LAS u32x2*)(uintptr_t)a; }
DI u32x4 lds128u(unsigned a) { return *(const LAS u32x4*)(uintptr_t)a; }
DI s16x4 ldstr(unsigned a) { return __builtin_amdgcn_ds_read_tr16_b64_v4i16((LAS s16x4*)(uintptr_t)a); }
DI bf16x8 ldstr2(unsigned a0, unsigned a1) {
  s16x4 lo = ldstr(a0), hi = ldstr(a1);
  return __builtin_bit_cast(bf16x8, __builtin_shufflevector(lo, hi, 0, 1, 2, 3, 4, 5, 6, 7));
}
DI f32x4 mfma32(bf16x8 a, bf16x8 b, f32x4 c) { return __builtin_amdgcn_mfma_f32_16x16x32_bf16(a, b, c, 0, 0, 0); }
DI f32x4 mfma16(u32x2 a, u32x2 b, f32x4 c) {
  return __builtin_amdgcn_mfma_f32_16x16x16bf16_1k(__builtin_bit_cast(s16x4, a), __builtin_bit_cast(s16x4, b), c, 0, 0, 0);
}
DI bf16x8 asbf(u32x4 v) { return __builtin_bit_cast(bf16x8, v); }
DI unsigned pk2(float a, float b) { f32x2 p = {a, b}; return __builtin_bit_cast(unsigned, __builtin_convertvector(p, bf16x2)); }
DI u32x2 pk4(f32x4 v) { u32x2 r = {pk2(v[0], v[1]), pk2(v[2], v[3])}; return r; }
DI float bflo(unsigned u) { return __uint_as_float(u << 16); }
DI float bfhi(unsigned u) { return __uint_as_float(u & 0xffff0000u); }
DI f32x4 gelu4(f32x4 x) {
  constexpr float C0 = -2.0f * 0.7978845608028654f * 1.4426950408889634f, C1 = C0 * 0.044715f;
  f32x4 t = (x * x * C1 + C0) * x, r;
#pragma unroll
  for (int k = 0; k < 4; ++k) r[k] = __builtin_amdgcn_rcpf(1.0f + __builtin_amdgcn_exp2f(t[k]));
  return x * r;
}
template <int CTRL> DI float dpp_mov(float x) {
  return __builtin_bit_cast(float, __builtin_amdgcn_update_dpp(0, __builtin_bit_cast(int, x), CTRL, 0xf, 0xf, true));
}
DI float red8x16(const float (&v)[8], int c16) {
  const bool b0 = c16 & 1, b1 = c16 & 2, b2 = c16 & 4;
  float a[4], b[2];
#pragma unroll
  for (int k = 0; k < 4; ++k) { float keep = b0 ? v[k + 4] : v[k], give = b0 ? v[k] : v[k + 4]; a[k] = keep + dpp_mov<0xB1>(give); }
#pragma unroll
  for (int k = 0; k < 2; ++k) { float keep = b1 ? a[k + 2] : a[k], give = b1 ? a[k] : a[k + 2]; b[k] = keep + dpp_mov<0x4E>(give); }
  float keep = b2 ? b[1] : b[0], give = b2 ? b[0] : b[1];
  float r = keep + dpp_mov<0x141>(dpp_mov<0x1B>(give));
  r = r + dpp_mov<0x140>(dpp_mov<0x141>(r));
  return r;
}
DI float xq_max(float x) {
  auto a = __builtin_amdgcn_permlane16_swap(__float_as_uint(x), __float_as_uint(x), false, false);
  x = fmaxf(__uint_as_float(a[0]), __uint_as_float(a[1]));
  auto b = __builtin_amdgcn_permlane32_swap(__float_as_uint(x), __float_as_uint(x), false, false);
  return fmaxf(__uint_as_float(b[0]), __uint_as_float(b[1]));
}
DI float xq_sum(float x) {
  auto a = __builtin_amdgcn_permlane16_swap(__float_as_uint(x), __float_as_uint(x), false, false);
  x = __uint_as_float(a[0]) + __uint_as_float(a[1]);
  auto b = __builtin_amdgcn_permlane32_swap(__float_as_uint(x), __float_as_uint(x), false, false);
  return __uint_as_float(b[0]) + __uint_as_float(b[1]);
}

DI void st128(srd_t s, unsigned off, u32x4 v) { __builtin_amdgcn_raw_buffer_store_b128(v, s, off, 0, 16); }
DI void st64(srd_t s, unsigned off, u32x2 v) { __builtin_amdgcn_raw_buffer_store_b64(v, s, off, 0, 16); }
DI u32x4 ld128(srd_t s, unsigned voff, unsigned soff) { return __builtin_amdgcn_raw_buffer_load_b128(s, voff, soff, 16); }

DI void set_flag(char* ws, int idx, u64 tag) {
  __hip_atomic_store((GAS u64*)(ws + WS_FLAGS + (size_t)idx * FLAG_STRIDE), tag, __ATOMIC_RELAXED, __HIP_MEMORY_SCOPE_AGENT);
}
DI void wait_flags(char* ws, int idx, u64 tag) {
  GAS u64* p = (GAS u64*)(ws + WS_FLAGS + (size_t)(idx < 0 ? 0 : idx) * FLAG_STRIDE);
#pragma nounroll
  for (unsigned spin = 0; spin < SPIN_CAP; ++spin) {
    u64 v = __hip_atomic_load(p, __ATOMIC_RELAXED, __HIP_MEMORY_SCOPE_AGENT);
    if (__all(idx < 0 || v == tag)) break;
    __builtin_amdgcn_s_sleep(1);
  }
  asm volatile("" ::: "memory");
}

DI void xmap(int mp, int slot, int T2, int& R, int& c) {
  int m = (mp + T2) & 15, par = (slot >> 3) ^ (m >> 3);
  R = 2 * mp + par; c = (slot & 7) ^ ((m & 7) ^ (par << 1));
}
DI unsigned xaddr(int R, int T, int c) {
  int tau = (R + T) & 31, par = tau & 1, m = tau >> 1;
  return (unsigned)((R >> 1) * 256 + 16 * ((8 * par + c) ^ (8 * (m >> 3) + ((m & 7) ^ (par << 1)))));
}
DI unsigned rec_sum(unsigned a, unsigned b, unsigned thi) { return thi ^ a ^ ((b << 13) | (b >> 19)); }

constexpr int P2D = 8, P2_POLL = 20, P2_CHK = 26;
struct P2Sched { int wait[32]; int w_poll; int w_epi; };
constexpr int p2_hi(int s) { int h = P2R + 2 * (s + 1); if (h > s + 1 + P2D) h = s + 1 + P2D; return h > 32 ? 32 : h; }
constexpr P2Sched make_p2(int NF) {
  P2Sched z{};
  int seq[256] = {}; int n = 0;
  for (int s = 0; s < P2R; ++s) for (int f = 0; f < NF; ++f) seq[n++] = s;
  int hi = P2R;
  for (int s = 0; s < 32; ++s) {
    int last = -1;
    for (int t = 0; t < n; ++t) if (seq[t] == s) last = t;
    z.wait[s] = n - 1 - last;
    if (s == P2_CHK) { int lp = -1; for (int t = 0; t < n; ++t) if (seq[t] == 100) lp = t; z.w_poll = n - 1 - lp; }
    for (; hi < p2_hi(s); ++hi) for (int f = 0; f < NF; ++f) seq[n++] = hi;
    if (s == P2_POLL) seq[n++] = 100;
    if (s == P2_CHK) { seq[n++] = 101; seq[n++] = 101; for (int f = 0; f < 2 * NF; ++f) seq[n++] = 101; }
    if (s >= 27 && s <= 30) { seq[n++] = 102; seq[n++] = 102; }
    if (s == 31) seq[n++] = 102;
  }
  int last = -1; for (int t = 0; t < n; ++t) if (seq[t] == 101) last = t;
  z.w_epi = n - 1 - last;
  return z;
}
constexpr P2Sched P2S[2] = {make_p2(1), make_p2(2)};

__global__ __launch_bounds__(512) __attribute__((amdgpu_waves_per_eu(2, 2)))
void mk_kernel(const __hip_bfloat16* __restrict__ x_, const __hip_bfloat16* __restrict__ wqkv_,
               const __hip_bfloat16* __restrict__ wo_, const __hip_bfloat16* __restrict__ win_,
               const __hip_bfloat16* __restrict__ wsp_, const __hip_bfloat16* __restrict__ bsp_,
               const __hip_bfloat16* __restrict__ wout_, const __hip_bfloat16* __restrict__ gw_,
               __hip_bfloat16* __restrict__ out_, char* __restrict__ ws) {
  extern __shared__ __attribute__((aligned(16))) char smem[];
  const unsigned L0 = (unsigned)(uintptr_t)(LAS char*)smem;
  const int tid = threadIdx.x, lane = tid & 63;
  const int w = (int)rfl((unsigned)tid >> 6);
  const int c16 = lane & 15, q4 = lane >> 4;
  const int tq = (lane & 15) >> 2, tp = lane & 3;
  const int bid = blockIdx.x, xcd = bid & 7, rr = bid >> 3;

  {
    u64 pc; unsigned len;
    asm volatile("s_getpc_b64 %0\n\tmk_code_begin:\n\ts_mov_b32 %1, mk_code_end-mk_code_begin" : "=s"(pc), "=s"(len));
#define CPL 2
    const unsigned off = (unsigned)(((rr * 8 + w) * CPL + lane) * 128) & 0xffffu;
    if (lane < CPL && off < len) {
      const char* p = (const char*)pc + off;
      asm volatile("s_mov_b32 m0, %0\n\ts_nop 3\n\tglobal_load_lds_dword %1, off" :: "s"(L0 + 162560u), "v"(p) : "m0", "memory");
    }
  }
  const srd_t sX = __builtin_amdgcn_make_buffer_rsrc((void*)x_, 0, 2048 * 512 * 2, 0x00020000);
  const srd_t sWS = __builtin_amdgcn_make_buffer_rsrc((void*)ws, 0, WS_END, 0x00020000);
  const srd_t sWSP = __builtin_amdgcn_make_buffer_rsrc((void*)wsp_, 0, 2048 * 2048 * 2, 0x00020000);
  u32x2 genv;
  ald64(genv, sWS, WS_GEN, 0);

  const int p2_q = xcd & 1, p2_i = 8 * (xcd >> 1) + (rr & 7), p2_J = ((rr >> 3) + 2) & 3;
  unsigned wsp_voff;
  { int R, c; xmap(4 * w + q4, c16, 0, R, c); wsp_voff = (unsigned)((64 * p2_i + R) * 4096 + p2_q * 2048 + c * 16); }
  auto wsp_issue = [&](int kk) { glds(sWSP, wsp_voff, (unsigned)kk * 128u, L0 + (unsigned)kk * 8192u + (unsigned)w * 1024u); };
  constexpr unsigned LDUMP = 162560, LCNT = 163824;
  const srd_t sWO = __builtin_amdgcn_make_buffer_rsrc((void*)wo_, 0, 512 * 512 * 2, 0x00020000);
  const srd_t sWOUT = __builtin_amdgcn_make_buffer_rsrc((void*)wout_, 0, 688 * 512 * 2, 0x00020000);
  if (tid == 0) *(LAS u32x4*)(uintptr_t)(L0 + LCNT) = u32x4{0u, 0u, 0u, 0u};
  auto arrive_flag = [&](int which, int flag, u64 tg) {
    if (lane == 0) {
      const unsigned old = __hip_atomic_fetch_add((LAS unsigned*)(uintptr_t)(L0 + LCNT + 4u * which), 1u, __ATOMIC_RELAXED, __HIP_MEMORY_SCOPE_WORKGROUP);
      if (old == 7u) set_flag(ws, flag, tg);
    }
  };
  const unsigned zf_off = WS_FLAGS + (unsigned)(F_Z2 + (8 * p2_q + (lane & 7)) * 8 + ((2 * p2_J + 7 + (lane < 24 ? lane >> 3 : 0)) & 7)) * FLAG_STRIDE;
  u32x2 zpre;
  u64 tag;
  int late_flag;
  const int i1 = 8 * (xcd & 1) + (rr & 7), cc = rr >> 3, cg = 2 * (xcd >> 1) + (cc & 1);
#define ROTM 2
  const int rot = ROTM == 0 ? 0 : (ROTM == 1 ? 4 * ((i1 + cc) & 1) : 2 * ((i1 + cc) & 3));
  const bool zwg = cc < 2;

  {
    if (zwg) {
      const int j = cg;
      late_flag = F_Z1 + i1 * 8 + j;
      const srd_t sWIN = __builtin_amdgcn_make_buffer_rsrc((void*)win_, 0, 512 * 1376 * 2, 0x00020000);
      constexpr unsigned SL = 28672, LZ1 = 62464, LSC = 160768;
      const int rp = w & 3, ch = w >> 2;
      unsigned xv0, xv1, wv2a, wv2b, wv1a, wv1b, wv1c;
      { int R, c; xmap(4 * w + q4, c16, 0, R, c); xv0 = (unsigned)((128 * i1 + R) * 1024 + c * 16);
        xmap(4 * (w + 8) + q4, c16, 0, R, c); xv1 = (unsigned)((128 * i1 + R) * 1024 + c * 16); }
      {
        auto wmap = [&](int piece, int col0) {
          int n = 64 * piece + lane, row = n / 12, ccx = n % 12, cb = (ccx >> 1) ^ ((row >> 3) & 1);
          return (unsigned)(row * 2752 + (col0 + 16 * cb + 8 * (ccx & 1)) * 2);
        };
        const int c2 = j < 7 ? 704 + 96 * j : 688, c1 = 96 * j;
        wv2a = wmap(w, c2); wv2b = wmap(8 + rp, c2); wv1a = wmap(w, c1); wv1b = wmap(8 + w, c1); wv1c = wmap(16 + w, c1);
      }
      auto dmaS = [&](int s, int part) {
        const unsigned base = L0 + (unsigned)(s % 3) * SL, ph = (unsigned)((s + rot) & 7);
        if (part == 0) { if (!ch) glds(sWIN, wv2b, ph * (64u * 2752u), base + 16384u + (unsigned)(8 + rp) * 1024u); }
        if (part == 1) glds(sWIN, wv2a, ph * (64u * 2752u), base + 16384u + (unsigned)w * 1024u);
        if (part == 2) glds(sX, xv0, ph * 128u, base + (unsigned)w * 1024u);
        if (part == 3) glds(sX, xv1, ph * 128u, base + (unsigned)(w + 8) * 1024u);
      };
      auto dmaZ1 = [&](int n) {
        const int k = n / 3, e = n % 3;
        const unsigned base = L0 + LZ1 + (unsigned)k * 24576u + (unsigned)w * 1024u, so = (unsigned)((k + (rot >> 1)) & 3) * (128u * 2752u);
        glds(sWIN, e == 0 ? wv1a : (e == 1 ? wv1b : wv1c), so, base + (unsigned)e * 8192u);
      };
#pragma unroll
      for (int s = 0; s < 2; ++s)
#pragma unroll
        for (int p = 0; p < 4; ++p) dmaS(s, p);

      unsigned xa[2][2];
#pragma unroll
      for (int b = 0; b < 2; ++b)
#pragma unroll
        for (int ks = 0; ks < 2; ++ks) xa[b][ks] = L0 + xaddr(32 * rp + 2 * c16 + b, 0, 4 * ks + q4);
      unsigned wa[3];
#pragma unroll
      for (int cb = 0; cb < 3; ++cb) wa[cb] = L0 + (unsigned)((8 * q4 + tq) * 192 + 32 * ((3 * ch + cb) ^ (q4 & 1)) + 8 * tp);

      f32x4 az[2][3], a1[3][2];
#pragma unroll
      for (int b = 0; b < 2; ++b)
#pragma unroll
        for (int cb = 0; cb < 3; ++cb) { az[b][cb] = f32x4{0, 0, 0, 0}; a1[cb][b] = f32x4{0, 0, 0, 0}; }
      const int nv = j < 7 ? 3 : (ch == 0 ? 1 : 0);
      bf16x8 xf[16][2];
      bf16x8 wz[2][2][3];
      auto rdx = [&](auto S_, int ks) { constexpr int s = decltype(S_)::v; constexpr unsigned so = (unsigned)(s % 3) * SL;
        xf[2 * s + ks][0] = lds128(xa[0][ks] + so); xf[2 * s + ks][1] = lds128(xa[1][ks] + so); };
      auto rdw = [&](auto S_, int ks, int cb) { constexpr int s = decltype(S_)::v; constexpr unsigned so = (unsigned)(s % 3) * SL + 16384u;
        wz[s & 1][ks][cb] = ldstr2(wa[cb] + so + ks * 6144, wa[cb] + so + ks * 6144 + 768); };
      wait_bar<3>();
#pragma unroll
      for (int ks = 0; ks < 2; ++ks) { rdx(IC<0>{}, ks); rdw(IC<0>{}, ks, 0); rdw(IC<0>{}, ks, 1); rdw(IC<0>{}, ks, 2); }
      SCHED();
#pragma unroll
      for (int p = 0; p < 4; ++p) dmaS(2, p);
      sfor<0, 8>([&](auto S_) {
        constexpr int s = decltype(S_)::v;
        if constexpr (s < 7) wait_bar<(s < 6 ? 3 : 4)>();
#pragma unroll
        for (int g = 0; g < 6; ++g) {
          const int ks = g / 3, cb = g % 3;
          az[0][cb] = mfma32(xf[2 * s + ks][0], wz[s & 1][ks][cb], az[0][cb]);
          az[1][cb] = mfma32(xf[2 * s + ks][1], wz[s & 1][ks][cb], az[1][cb]);
          if constexpr (s < 7) { if (cb == 0) rdx(IC<(s < 7 ? s + 1 : 0)>{}, ks); rdw(IC<(s < 7 ? s + 1 : 0)>{}, ks, cb); }
          SCHED();
          if constexpr (s + 3 < 8) { if (g == 0) dmaS(s + 3, 0); if (g == 1) dmaS(s + 3, 1); if (g == 3) dmaS(s + 3, 2); if (g == 4) dmaS(s + 3, 3); }
          else { if (g == 0) dmaZ1(4 * (s - 5)); if (g == 1) dmaZ1(4 * (s - 5) + 1); if (g == 3) dmaZ1(4 * (s - 5) + 2); if (g == 4) dmaZ1(4 * (s - 5) + 3); }
          SCHED();
        }
      });
      bar();
      pin(genv);
      tag = (((u64)genv[1] << 32) | genv[0]) + 1ull;
      const unsigned tlo = (unsigned)tag, thi = (unsigned)(tag >> 32);
      if (ch) {
#pragma unroll
        for (int kk = 0; kk < 7; ++kk) wsp_issue(kk);
      }
      SCHED();
      {
        float s1[8], s2[8];
#pragma unroll
        for (int b = 0; b < 2; ++b)
#pragma unroll
          for (int cb = 0; cb < 3; ++cb) az[b][cb] = gelu4(az[b][cb]);
        const float en = nv == 3 ? 1.0f : 0.0f;
#pragma unroll
        for (int b = 0; b < 2; ++b) {
          const f32x4 e1 = az[b][1] * en, e2 = az[b][2] * en;
          const f32x4 t1 = az[b][0] + e1 + e2, t2 = az[b][0] * az[b][0] + e1 * e1 + e2 * e2;
#pragma unroll
          for (int r = 0; r < 4; ++r) { s1[2 * r + b] = t1[r]; s2[2 * r + b] = t2[r]; }
        }
        const float sm = red8x16(s1, c16), sv = red8x16(s2, c16);
        const int kidx = 4 * (c16 & 1) + (c16 & 2) + ((c16 >> 2) & 1);
        unsigned so = WS_STATS + (unsigned)(((i1 * 16 + 2 * j + ch) * 128 + 32 * rp + 8 * q4 + kidx) * 16);
        if (c16 >= 8) so = OOB;
        const unsigned a = __float_as_uint(sm), b = __float_as_uint(sv);
        st128(sWS, so, u32x4{a, b, tlo, rec_sum(a, b, thi)});
      }
      SCHED();
      if (!ch) {
#pragma unroll
        for (int kk = 0; kk < 7; ++kk) wsp_issue(kk);
      }
      if (j == 7) {
        const int row = tid & 63;
        const unsigned short gv = __builtin_bit_cast(unsigned short, gw_[(((tid >> 6) + rot) & 7) * 64 + row]);
        wait_bar<0>();
        *(LAS unsigned short*)(uintptr_t)(L0 + LZ1 + (unsigned)(tid >> 6) * 12288u + (unsigned)(row * 192 + 32 * (3 ^ ((row >> 3) & 1)))) = gv;
      }
      wait_bar<8>();
      constexpr int RECU = RECU_, ARRU = ARRU_;
      static_assert(RECU < 8 && ARRU > 8 && ARRU <= 14, "");
      u32x4 rec[8];
      const unsigned rec_off = WS_STATS + (unsigned)(((i1 * 16 + 8 * (lane >> 5)) * 128 + 32 * rp + (lane & 31)) * 16);
      bf16x8 wf[2][3];
      unsigned wb0[3], wb1[3];
#pragma unroll
      for (int cb = 0; cb < 3; ++cb) { wb0[cb] = wa[cb] + LZ1; wb1[cb] = wa[cb] + LZ1 + 8u * 6144u;
        pin(wb0[cb]); pin(wb1[cb]);
      }
#pragma unroll
      for (int cb = 0; cb < 3; ++cb) wf[0][cb] = ldstr2(wb0[cb], wb0[cb] + 768);
      sfor<0, 16>([&](auto U_) {
        constexpr int u = decltype(U_)::v;
        if constexpr (u == 8) { bar(); }
        if constexpr (u + 1 < 16) {
          constexpr unsigned wo = (unsigned)((u + 1) & 7) * 6144u;
#pragma unroll
          for (int cb = 0; cb < 3; ++cb) { const unsigned bb_ = (u + 1 < 8 ? wb0[cb] : wb1[cb]) + wo; wf[(u + 1) & 1][cb] = ldstr2(bb_, bb_ + 768); }
        }
#pragma unroll
        for (int cb = 0; cb < 3; ++cb) {
          a1[cb][0] = mfma32(wf[u & 1][cb], xf[u][0], a1[cb][0]);
          a1[cb][1] = mfma32(wf[u & 1][cb], xf[u][1], a1[cb][1]);
        }
        SCHED();
        if constexpr (u >= RECU - 5 && u < RECU - 1) { ald128(rec[2 * (u - RECU + 5)], sWS, rec_off, (unsigned)(2 * (u - RECU + 5)) * 2048u); ald128(rec[2 * (u - RECU + 5) + 1], sWS, rec_off, (unsigned)(2 * (u - RECU + 5) + 1) * 2048u); }
        if constexpr (u == ARRU) { wait_vm<ARRU - 8>(); arrive_flag(2, F_Z2 + i1 * 8 + j, tag); }
        if constexpr (u >= 8 && u < 14) wsp_issue(u - 1);
        SCHED();
        if constexpr (u == RECU) {
          wait_vm<0>();
#pragma unroll
          for (int k = 0; k < 8; ++k) pin(rec[k]);
#pragma nounroll
          for (unsigned spin = 0; spin < SPIN_CAP; ++spin) {
            bool ok = true;
#pragma unroll
            for (int k = 0; k < 8; ++k) ok &= (rec[k][2] == tlo && rec[k][3] == rec_sum(rec[k][0], rec[k][1], thi));
            if (__all(ok)) break;
            __builtin_amdgcn_s_sleep(1);
#pragma unroll
            for (int k = 0; k < 8; ++k) rec[k] = ld128(sWS, rec_off, (unsigned)k * 2048u);
          }
          if (lane >= 32) { rec[7][0] = 0u; rec[7][1] = 0u; }
          float t1 = 0.f, t2 = 0.f;
#pragma unroll
          for (int k = 0; k < 8; ++k) { t1 += __uint_as_float(rec[k][0]); t2 += __uint_as_float(rec[k][1]); }
          { auto e = __builtin_amdgcn_permlane32_swap(__float_as_uint(t1), __float_as_uint(t1), false, false); t1 = __uint_as_float(e[0]) + __uint_as_float(e[1]); }
          { auto e = __builtin_amdgcn_permlane32_swap(__float_as_uint(t2), __float_as_uint(t2), false, false); t2 = __uint_as_float(e[0]) + __uint_as_float(e[1]); }
          const float mean = t1 * (1.0f / 688.0f);
          const float var = fmaxf(t2 * (1.0f / 688.0f) - mean * mean, 0.f);
          const float rstd = __builtin_amdgcn_rsqf(var + 1e-5f);
          const unsigned sc = L0 + LSC + (unsigned)(w & 3) * 256u;
          if (lane < 32) lds_st64(sc + (unsigned)lane * 8u, u32x2{__float_as_uint(rstd), __float_as_uint(-mean * rstd)});
          asm volatile("s_waitcnt lgkmcnt(0)" ::: "memory");
          f32x4 st[4];
#pragma unroll
          for (int r = 0; r < 4; ++r) st[r] = lds128f(sc + (unsigned)(8 * q4 + 2 * r) * 8u);
          const int fb2 = j < 7 ? 1 + 6 * j + 3 * ch : 0;
#pragma unroll
          for (int cb = 0; cb < 3; ++cb) {
            u32x4 o;
#pragma unroll
            for (int r = 0; r < 4; ++r) o[r] = pk2(__builtin_fmaf(az[0][cb][r], st[r][0], st[r][1]), __builtin_fmaf(az[1][cb][r], st[r][2], st[r][3]));
            unsigned zo = WS_Z2N + (unsigned)((((4 * i1 + rp) * 44 + fb2 + cb) * 64 + lane) * 16);
            if (cb >= nv) zo = OOB;
            st128(sWS, zo, o);
          }
          SCHED();
        }
      });
      bar();
      if (ch) { wsp_issue(13); wsp_issue(14); wsp_issue(15); }
      SCHED();
      {
        const float g0 = __builtin_amdgcn_rcpf(1.0f + __builtin_amdgcn_exp2f(-1.4426950408889634f * a1[0][0][0]));
        const float g1 = __builtin_amdgcn_rcpf(1.0f + __builtin_amdgcn_exp2f(-1.4426950408889634f * a1[0][1][0]));
        st64(sWS, (q4 == 0 && nv == 0) ? WS_GATE + (unsigned)((128 * i1 + 32 * rp + 2 * c16) * 4) : OOB, u32x2{__float_as_uint(g0), __float_as_uint(g1)});
        const unsigned z1o = WS_Z1 + (unsigned)((((4 * i1 + rp) * 44 + 6 * j + 3 * ch) * 64 + lane) * 16);
#pragma unroll
        for (int cb = 0; cb < 3; ++cb) {
          const u32x2 p0 = pk4(gelu4(a1[cb][0])), p1 = pk4(gelu4(a1[cb][1]));
          if (cb == 2) { SCHED(); ald64(zpre, sWS, zf_off, 0); SCHED(); }
          st128(sWS, cb < nv ? z1o + (unsigned)cb * 1024u : OOB, u32x4{p0[0], p0[1], p1[0], p1[1]});
        }
      }
      SCHED();
      if (!ch) { wsp_issue(13); wsp_issue(14); wsp_issue(15); wait_vm<4>(); } else wait_vm<1>();
      pin(zpre);
    } else {
      const int hg = cg;
      late_flag = F_O + i1 * 8 + hg;
      const srd_t sWQ = __builtin_amdgcn_make_buffer_rsrc((void*)wqkv_, 0, 512 * 1536 * 2, 0x00020000);
      constexpr unsigned SLOT = 43008, LWQ = 18432, LSC = 131072;
      const int h = w & 3, th = w >> 2;
      unsigned xv0, xv1, xvh, wv;
      { int R, c, t;
        xmap(4 * (2 + w) + q4, c16, 8, R, c); xv0 = (unsigned)((128 * i1 - 16 + R) * 1024 + c * 16);
        xmap(4 * (10 + w) + q4, c16, 8, R, c); xv1 = (unsigned)((128 * i1 - 16 + R) * 1024 + c * 16);
        xmap(w, lane >> 2, 8, R, c); t = 128 * i1 - 16 + R; if (t < 0) t = 0; xvh = (unsigned)(t * 1024 + c * 16 + (lane & 3) * 4); }
      { int row = 8 * w + (lane >> 3), cx = lane & 7, f = ((row >> 1) & 1) | ((row >> 2) & 2);
        int col = 64 * hg + 16 * ((cx >> 1) ^ f) + 8 * (cx & 1);
        wv = (unsigned)(row * 3072 + col * 2); }
      auto dmaQ = [&](int s, int part) {
        const unsigned base = L0 + (unsigned)(s % 3) * SLOT, ph = (unsigned)((s + rot) & 7);
        if (part == 0) glds(sX, xv0, ph * 128u, base + (unsigned)(2 + w) * 1024u);
        if (part == 1) glds(sX, xv1, ph * 128u, base + (unsigned)(10 + w) * 1024u);
        if (part == 2) glds4(sX, xvh, ph * 128u, base + (unsigned)w * 256u);
        if (part >= 3) glds(sWQ, wv, ph * (64u * 3072u) + (unsigned)(part - 3) * 1024u, base + LWQ + (unsigned)(part - 3) * 8192u + (unsigned)w * 1024u);
      };
#pragma unroll
      for (int s = 0; s < 2; ++s)
#pragma unroll
        for (int p = 0; p < 6; ++p) dmaQ(s, p);
      unsigned xa[2][2], xh[2];
#pragma unroll
      for (int ks = 0; ks < 2; ++ks) {
#pragma unroll
        for (int par = 0; par < 2; ++par) xa[par][ks] = L0 + xaddr(16 + 64 * th + 2 * c16 + par, 16, 4 * ks + q4);
        xh[ks] = L0 + xaddr(c16, 16, 4 * ks + q4);
      }
      const unsigned wa = L0 + LWQ + (unsigned)((8 * q4 + tq) * 128 + 32 * (h ^ ((tq >> 1) | ((q4 & 1) << 1))) + 8 * tp);
      f32x4 aq[4], ak[5], av[5];
#pragma unroll
      for (int n = 0; n < 4; ++n) aq[n] = f32x4{0, 0, 0, 0};
#pragma unroll
      for (int n = 0; n < 5; ++n) { ak[n] = f32x4{0, 0, 0, 0}; av[n] = f32x4{0, 0, 0, 0}; }
      auto qloop = [&](auto HALO_) {
        constexpr bool HALO = decltype(HALO_)::v != 0;
        bf16x8 wf[2][2][3], xf[2][2][5];
        auto rdw = [&](auto S_, int ks, int sg) { constexpr int s = decltype(S_)::v; constexpr unsigned so = (unsigned)(s % 3) * SLOT;
          wf[s & 1][ks][sg] = ldstr2(wa + so + sg * 8192 + ks * 4096, wa + so + sg * 8192 + ks * 4096 + 512); };
        auto rdx = [&](auto S_, int ks, int n) { constexpr int s = decltype(S_)::v; constexpr unsigned so = (unsigned)(s % 3) * SLOT;
          if (n < 4) xf[s & 1][ks][n] = lds128(xa[n & 1][ks] + so + (unsigned)(n >> 1) * 4096u); else if constexpr (HALO) xf[s & 1][ks][4] = lds128(xh[ks] + so); };
        wait_bar<6>();
#pragma unroll
        for (int ks = 0; ks < 2; ++ks) {
#pragma unroll
          for (int k = 0; k < 3; ++k) rdw(IC<0>{}, ks, k);
#pragma unroll
          for (int n = 0; n < 5; ++n) rdx(IC<0>{}, ks, n);
        }
        SCHED();
#pragma unroll
        for (int p = 0; p < 6; ++p) dmaQ(2, p);
          sfor<0, 8>([&](auto S_) {
          constexpr int s = decltype(S_)::v;
          if constexpr (s < 7) wait_bar<(s <= 1 ? 6 : (s == 2 ? 7 : (s <= 5 ? 8 : 6)))>();
          constexpr int c = s & 1;
#pragma unroll
          for (int g = 0; g < 8; ++g) {
            const int ks = g >> 2, n = g & 3;
            aq[n] = mfma32(wf[c][ks][0], xf[c][ks][n], aq[n]);
            ak[n] = mfma32(wf[c][ks][1], xf[c][ks][n], ak[n]);
            av[n] = mfma32(xf[c][ks][n], wf[c][ks][2], av[n]);
            if constexpr (HALO) { if (n == 3) { ak[4] = mfma32(wf[c][ks][1], xf[c][ks][4], ak[4]); av[4] = mfma32(xf[c][ks][4], wf[c][ks][2], av[4]); } }
            if constexpr (s < 7) { rdx(IC<(s < 7 ? s + 1 : 0)>{}, ks, n); if (n < 3) rdw(IC<(s < 7 ? s + 1 : 0)>{}, ks, n); else rdx(IC<(s < 7 ? s + 1 : 0)>{}, ks, 4); }
            SCHED();
            if constexpr (s + 3 < 8) { if (g < 6) dmaQ(s + 3, g); }
            if constexpr (s >= 1 && s <= 4) { if (g == 6)
              glds4(sWSP, lane < 16 ? (unsigned)((64 * p2_i + 32 * (cc & 1) + 8 * (s - 1) + w) * 4096 + p2_q * 2048 + lane * 128) : OOB, 0, L0 + LDUMP); }
            if constexpr (s == 5) { if (g < 5) wsp_issue(11 + g); }
            if constexpr (s == 6) { if (g < 5) wsp_issue(g); }
            if constexpr (s == 7) { if (g < 6) wsp_issue(5 + g); }
            SCHED();
          }
          if constexpr (s == 6) bar();
        });
      };
      if (th == 0) qloop(IC<1>{}); else qloop(IC<0>{});
      pin(genv);
      tag = (((u64)genv[1] << 32) | genv[0]) + 1ull;
      u32x2 qb[4], kb[5], vb[5];
#pragma unroll
      for (int n = 0; n < 4; ++n) qb[n] = pk4(aq[n]);
#pragma unroll
      for (int n = 0; n < 5; ++n) { kb[n] = pk4(ak[n]); vb[n] = pk4(av[n]); }
      const unsigned ex = L0 + LSC + (unsigned)h * 2048u + (unsigned)lane * 8u;
      if (th == 0) { lds_st64(ex, kb[2]); lds_st64(ex + 512, kb[3]); lds_st64(ex + 1024, vb[2]); lds_st64(ex + 1536, vb[3]); }
      bar();
      u32x2 kp[2], vp[2];
      if (th == 0) { kp[0] = kb[4]; kp[1] = kb[4]; vp[0] = vb[4]; vp[1] = vb[4]; }
      else { kp[0] = lds64(ex); kp[1] = lds64(ex + 512); vp[0] = lds64(ex + 1024); vp[1] = lds64(ex + 1536); }
      constexpr float NEG = -1e30f;
      f32x4 bias_c, bias_p, bias_f[2];
      unsigned mc[2], mp_[2], mf[2][2];
      {
        unsigned vc = 0, vpv = 0, vh0 = 0, vh1 = 0;
#pragma unroll
        for (int r = 0; r < 4; ++r) {
          const int kap = 4 * q4 + r;
          const bool c_ok = (kap <= c16) && (kap >= c16 - 7);
          const bool p_ok = kap >= c16 + 9;
          const bool h0 = th ? p_ok : ((i1 > 0) && ((kap & 1) == 0) && (kap >= 2 * c16 + 2));
          const bool h1 = th ? p_ok : ((i1 > 0) && ((kap & 1) == 1) && (kap >= 2 * c16 + 3));
          bias_c[r] = c_ok ? 0.f : NEG; bias_p[r] = p_ok ? 0.f : NEG; bias_f[0][r] = h0 ? 0.f : NEG; bias_f[1][r] = h1 ? 0.f : NEG;
          vc |= (unsigned)c_ok << r; vpv |= (unsigned)p_ok << r; vh0 |= (unsigned)h0 << r; vh1 |= (unsigned)h1 << r;
        }
        auto mk = [](unsigned v, int k) { return ((v >> (2 * k)) & 1u ? 0xffffu : 0u) | ((v >> (2 * k + 1)) & 1u ? 0xffff0000u : 0u); };
#pragma unroll
        for (int k = 0; k < 2; ++k) { mc[k] = mk(vc, k); mp_[k] = mk(vpv, k); mf[0][k] = mk(vh0, k); mf[1][k] = mk(vh1, k); }
      }
      constexpr float C2 = 0.25f * 1.4426950408889634f;
#pragma unroll
      for (int n = 0; n < 4; ++n) {
        const bool first = n < 2;
        const u32x2 kprev = first ? kp[n & 1] : kb[n - 2], vprev = first ? vp[n & 1] : vb[n - 2];
        const f32x4 bp = first ? bias_f[n & 1] : bias_p;
        const unsigned mp0 = first ? mf[n & 1][0] : mp_[0], mp1 = first ? mf[n & 1][1] : mp_[1];
        f32x4 sp = mfma16(kprev, qb[n], bp), sc = mfma16(kb[n], qb[n], bias_c);
        f32x4 sm;
#pragma unroll
        for (int r = 0; r < 4; ++r) sm[r] = fmaxf(sp[r], sc[r]);
        float m = xq_max(fmaxf(fmaxf(sm[0], sm[1]), fmaxf(sm[2], sm[3])));
        f32x4 p;
#pragma unroll
        for (int r = 0; r < 4; ++r) p[r] = __builtin_amdgcn_exp2f((sm[r] - m) * C2);
        const float inv = __builtin_amdgcn_rcpf(xq_sum((p[0] + p[1]) + (p[2] + p[3])));
#pragma unroll
        for (int r = 0; r < 4; ++r) p[r] *= inv;
        const u32x2 pp = pk4(p);
        u32x4 pb = {pp[0] & mp0, pp[1] & mp1, pp[0] & mc[0], pp[1] & mc[1]};
        u32x4 va = {vprev[0], vprev[1], vb[n][0], vb[n][1]};
        f32x4 o = mfma32(asbf(va), asbf(pb), f32x4{0, 0, 0, 0});
        const int tbl = 4 * th + n;
        lds_st64(L0 + LSC + 8192 + (unsigned)(((tbl * 2 + (h >> 1)) * 64 + lane) * 16 + (h & 1) * 8), pk4(o));
      }
      bar();
#pragma unroll
      for (int k = 0; k < 2; ++k) {
        const int f = w + 8 * k;
        u32x4 v = lds128u(L0 + LSC + 8192 + (unsigned)(f * 1024 + lane * 16));
        st128(sWS, WS_O + (unsigned)((((8 * i1 + (f >> 1)) * 16 + 2 * hg + (f & 1)) * 64 + lane) * 16), v);
      }
      zpre = u32x2{~(unsigned)tag, 0u};
      {
        const unsigned g = (unsigned)((rr & 15) * 512 + tid) * 128u;
        if ((rr & 15) < 8) glds4(sWO, g, 0, L0 + LDUMP); else glds4(sWOUT, g - 524288u, 0, L0 + LDUMP);
        glds4(sWOUT, g + 524288u, 0, L0 + LDUMP);
      }
    }
  }

  const int i3 = 4 * xcd + (rr & 3), j3 = rr >> 2;
  const int t2 = w & 1, kq = w >> 1;
  u32x4 of[4][2];
  u32x2 gg;
  unsigned p3_bv; int p3_uflag;
  {
    const int q = p2_q, i2 = p2_i, J = p2_J;
    const srd_t sBSP = __builtin_amdgcn_make_buffer_rsrc((void*)bsp_, 0, 2048 * 2, 0x00020000);
    const int n2 = J < 2 ? 4 : 2, fb0 = J < 2 ? 12 * J : 4 + 10 * J;
    const int fb = w < n2 ? fb0 + 2 * w : fb0 + n2 + w;
    const int nf = w < n2 ? 2 : (fb <= 42 ? 1 : 0);
    const int lf_idx = lane < 3 ? F_Z1 + (i2 >> 1) * 8 + ((2 * J + 7 + lane) & 7)
                                : (lane < 11 ? F_O + (i3 >> 1) * 8 + (lane - 3) : (lane == 11 ? F_Z1 + (i3 >> 1) * 8 + 7 : -1));
    GAS u64* zfp = (GAS u64*)(ws + zf_off);
    unsigned zmask;
    { const unsigned long long b = __ballot(((((u64)zpre[1]) << 32) | zpre[0]) == tag); zmask = (unsigned)(b & (b >> 8) & (b >> 16)) & 0xffu; }
    auto need = [&](unsigned bits) {
      if ((zmask & bits) == bits) return;
#pragma nounroll
      for (unsigned spin = 0; spin < SPIN_CAP; ++spin) {
        const u64 v = __hip_atomic_load(zfp, __ATOMIC_RELAXED, __HIP_MEMORY_SCOPE_AGENT);
        const unsigned long long b = __ballot(v == tag);
        zmask = (unsigned)(b & (b >> 8) & (b >> 16)) & 0xffu;
        if ((zmask & bits) == bits) break;
        __builtin_amdgcn_s_sleep(1);
      }
      asm volatile("" ::: "memory");
    };
    if (nf) need((1u << ((P2R + 3) / 4)) - 1u);
    unsigned ta[2][2];
#pragma unroll
    for (int par = 0; par < 2; ++par)
#pragma unroll
      for (int ks = 0; ks < 2; ++ks) ta[par][ks] = L0 + xaddr(2 * c16 + par, 0, 4 * ks + q4);
    const unsigned zv = WS_Z2N + (unsigned)(((32 * q) * 44 + fb) * 1024 + lane * 16);
    constexpr int rot2 = 0;
    const unsigned ov = WS_O + (unsigned)((((4 * i3 + 2 * t2) * 16 + 4 * kq) * 64 + lane) * 16);
    const unsigned gv = WS_GATE + (unsigned)((64 * i3 + 32 * t2 + 2 * c16) * 4);
    const unsigned z1v = WS_Z1 + (unsigned)((((2 * i2) * 44 + fb) * 64 + lane) * 16);
    f32x4 acc[2][4];
#pragma unroll
    for (int f = 0; f < 2; ++f)
#pragma unroll
      for (int t = 0; t < 4; ++t) acc[f][t] = f32x4{0, 0, 0, 0};
    unsigned bb[2];
    u32x4 z1f[2][2];
#pragma unroll
    for (int f = 0; f < 2; ++f) { z1f[f][0] = u32x4{0, 0, 0, 0}; z1f[f][1] = u32x4{0, 0, 0, 0}; }
    auto body = [&](auto NF_) {
      constexpr int NF = decltype(NF_)::v;
      constexpr int D = P2D;
      constexpr P2Sched SC = P2S[NF - 1];
      u32x4 zb[D][NF];
      u32x2 lfv;
      sfor<0, P2R>([&](auto S_) { constexpr int s = decltype(S_)::v;
#pragma unroll
        for (int f = 0; f < NF; ++f) ald128(zb[s][f], sWS, zv + f * 1024, (unsigned)((s + rot2) & 31) * 45056u); });
      wait_bar<P2R * NF>();
      if (tid == 0) set_flag(ws, late_flag, tag);
      bf16x8 tf[2][4];
#pragma unroll
      for (int t = 0; t < 4; ++t) tf[0][t] = lds128(ta[t & 1][0] + (unsigned)(t >> 1) * 4096u);
      sfor<0, 32>([&](auto S_) {
        constexpr int s = decltype(S_)::v;
        if constexpr (s + 1 < 32) {
          constexpr unsigned ko = (unsigned)((s + 1) >> 1) * 8192u;
#pragma unroll
          for (int t = 0; t < 4; ++t) tf[(s + 1) & 1][t] = lds128(ta[t & 1][(s + 1) & 1] + ko + (unsigned)(t >> 1) * 4096u);
        }
        wait_vm<SC.wait[s]>();
#pragma unroll
        for (int f = 0; f < NF; ++f) pin(zb[s % D][f]);
#pragma unroll
        for (int f = 0; f < NF; ++f)
#pragma unroll
          for (int t = 0; t < 4; ++t) acc[f][t] = mfma32(asbf(zb[s % D][f]), tf[s & 1][t], acc[f][t]);
        SCHED();
        sfor<(s == 0 ? P2R : p2_hi(s - 1)), p2_hi(s)>([&](auto H_) {
          constexpr int h = decltype(H_)::v;
          if constexpr ((h & 3) == 0 || h == P2R) need(1u << (h >> 2));
#pragma unroll
          for (int f = 0; f < NF; ++f) ald128(zb[h % D][f], sWS, zv + f * 1024, (unsigned)h * 45056u);
        });
        if constexpr (s == P2_POLL) ald64(lfv, sWS, lf_idx < 0 ? WS_FLAGS : WS_FLAGS + (unsigned)lf_idx * FLAG_STRIDE, 0);
        if constexpr (s == P2_CHK) {
          wait_vm<SC.w_poll>();
          pin(lfv);
          if (!__all(lf_idx < 0 || ((((u64)lfv[1] << 32) | lfv[0]) == tag))) wait_flags(ws, lf_idx, tag);
          ald32(bb[0], sBSP, (unsigned)((64 * i2 + 2 * c16) * 2), 0);
          ald32(bb[1], sBSP, (unsigned)((64 * i2 + 32 + 2 * c16) * 2), 0);
#pragma unroll
          for (int f = 0; f < NF; ++f) { ald128(z1f[f][0], sWS, z1v + f * 1024, 0); ald128(z1f[f][1], sWS, z1v + f * 1024, 45056u); }
        }
        if constexpr (s >= 27 && s <= 30) { ald128(of[s - 27][0], sWS, ov + (s - 27) * 1024, 0); ald128(of[s - 27][1], sWS, ov + (s - 27) * 1024, 16384u); }
        if constexpr (s == 31) ald64(gg, sWS, gv, 0);
        SCHED();
      });
      wait_vm<SC.w_epi>();
      pin(bb[0]); pin(bb[1]);
#pragma unroll
      for (int f = 0; f < NF; ++f) { pin(z1f[f][0]); pin(z1f[f][1]); }
    };
    if (nf == 2) body(IC<2>{});
    else if (nf == 1) body(IC<1>{});
    else {
      wait_bar<0>();
      wait_flags(ws, lf_idx, tag);
      bb[0] = 0; bb[1] = 0;
#pragma unroll
      for (int k = 0; k < 4; ++k) { ald128(of[k][0], sWS, ov + k * 1024, 0); ald128(of[k][1], sWS, ov + k * 1024, 16384u); }
      ald64(gg, sWS, gv, 0);
    }
    u32x2 ux0 = u32x2{0, 0}, ux1 = u32x2{0, 0}; unsigned uxo = OOB;
    {
      float bs[4];
#pragma unroll
      for (int g2 = 0; g2 < 2; ++g2) { bs[2 * g2] = q ? 0.f : bflo(bb[g2]); bs[2 * g2 + 1] = q ? 0.f : bfhi(bb[g2]); }
      u32x2 ub[2][4];
#pragma unroll
      for (int f = 0; f < 2; ++f)
#pragma unroll
        for (int t = 0; t < 4; ++t) {
          const u32x4 z = z1f[f][t >> 1];
          const int par = t & 1;
          f32x4 v;
          v[0] = bflo(z[2 * par]) * (acc[f][t][0] + bs[t]); v[1] = bfhi(z[2 * par]) * (acc[f][t][1] + bs[t]);
          v[2] = bflo(z[2 * par + 1]) * (acc[f][t][2] + bs[t]); v[3] = bfhi(z[2 * par + 1]) * (acc[f][t][3] + bs[t]);
          ub[f][t] = pk4(v);
        }
      const unsigned uo = WS_U + (unsigned)((((q * 128 + 4 * i2) * 22 + (fb >> 1)) * 64 + lane) * 16);
      if (w < n2) {
#pragma unroll
        for (int t = 0; t < 4; ++t) st128(sWS, uo + (unsigned)t * 22528u, u32x4{ub[0][t][0], ub[0][t][1], ub[1][t][0], ub[1][t][1]});
      } else {
        const unsigned xo = L0 + 155648u + (unsigned)(w - 2) * 1024u + (unsigned)lane * 16u;
        *(LAS u32x4*)(uintptr_t)xo = (w & 1) ? u32x4{ub[0][0][0], ub[0][0][1], ub[0][1][0], ub[0][1][1]} : u32x4{ub[0][2][0], ub[0][2][1], ub[0][3][0], ub[0][3][1]};
        ux0 = (w & 1) ? ub[0][2] : ub[0][0]; ux1 = (w & 1) ? ub[0][3] : ub[0][1];
        uxo = uo + ((w & 1) ? 2u * 22528u : 0u);
      }
    }
    const int brow = 8 * w + (lane >> 3), bcx = lane & 7;
    const unsigned bv = (unsigned)(brow * 1024 + (64 * j3 + 16 * ((bcx >> 1) ^ ((brow >> 1) & 3)) + 8 * (bcx & 1)) * 2);
#pragma unroll
    for (int n = 0; n < 3; ++n) glds(sWO, bv, (unsigned)n * 65536u, L0 + 131072u + (unsigned)(n * 8 + w) * 1024u);
    bar();
    if (w >= n2) {
      const u32x4 o = lds128u(L0 + 155648u + (unsigned)((w - 2) ^ 1) * 1024u + (unsigned)lane * 16u);
      const u32x4 v0 = (w & 1) ? u32x4{o[0], o[1], ux0[0], ux0[1]} : u32x4{ux0[0], ux0[1], o[0], o[1]};
      const u32x4 v1 = (w & 1) ? u32x4{o[2], o[3], ux1[0], ux1[1]} : u32x4{ux1[0], ux1[1], o[2], o[3]};
      st128(sWS, uxo, v0); st128(sWS, uxo + 22528u, v1);
    }
#pragma unroll
    for (int n = 3; n < 8; ++n) glds(sWO, bv, (unsigned)n * 65536u, L0 + (unsigned)((n - 3) * 8 + w) * 1024u);
    p3_bv = bv; p3_uflag = F_U + (i2 * 4 + J) * 2 + q;
  }

  {
    const int p0 = kq < 2 ? 6 * kq : 2 + 5 * kq, np = kq < 2 ? 6 : 5;
    const unsigned wb = (unsigned)((4 * q4 + tq) * 128 + 32 * (2 * (q4 & 1) + (tq >> 1)) + 8 * tp);
    f32x4 aa[4][2], ab[4][2];
#pragma unroll
    for (int cb = 0; cb < 4; ++cb)
#pragma unroll
      for (int t = 0; t < 2; ++t) { aa[cb][t] = f32x4{0, 0, 0, 0}; ab[cb][t] = f32x4{0, 0, 0, 0}; }
    wait_bar<0>();
    if (tid == 0) set_flag(ws, p3_uflag, tag);
#pragma unroll
    for (int k = 0; k < 4; ++k) { pin(of[k][0]); pin(of[k][1]); }
    pin(gg);
    sfor<0, 4>([&](auto K_) {
      constexpr int k = decltype(K_)::v;
      bf16x8 wf[4];
#pragma unroll
      for (int cb = 0; cb < 4; ++cb) {
        const unsigned a = (wb ^ (unsigned)(cb << 5)) + L0 + (4 * kq + k < 6 ? 131072u + (unsigned)(4 * kq + k) * 4096u : (unsigned)(4 * kq + k - 6) * 4096u);
        wf[cb] = ldstr2(a, a + 2048);
      }
#pragma unroll
      for (int cb = 0; cb < 4; ++cb) {
#pragma unroll
        for (int t = 0; t < 2; ++t) aa[cb][t] = mfma32(wf[cb], asbf(of[k][t]), aa[cb][t]);
        SCHED();
        const int n = 3 * k + cb;
        if (cb < 3 && n < 11) glds(sWOUT, p3_bv, (unsigned)n * 65536u, L0 + (unsigned)((n + 5) * 8 + w) * 1024u);
        SCHED();
      }
    });
    wait_bar<0>();
    wait_flags(ws, lane < 2 ? F_U + i3 * 8 + 2 * kq + lane : -1, tag);
    u32x4 uf[6][2][2];
    const unsigned uv = WS_U + (unsigned)((((4 * i3 + 2 * t2) * 22 + p0) * 64 + lane) * 16);
    const unsigned bbs = L0 + (unsigned)(10 + p0) * 4096u;
    auto bpart = [&](auto NP_) {
      constexpr int NP = decltype(NP_)::v;
      int pk[NP];
#pragma unroll
      for (int k = 0; k < NP; ++k) { int v = k + (UROT ? j3 % NP : 0); pk[k] = v >= NP ? v - NP : v; }
      constexpr int UD = UDEPTH;
      auto uld = [&](int k, int e) { ald128(uf[k][e >> 1][e & 1], sWS, uv, (unsigned)pk[k] * 1024u + (unsigned)((e >> 1) * 128 + (e & 1)) * 22528u); };
#pragma unroll
      for (int k = 0; k < UD; ++k)
#pragma unroll
        for (int e = 0; e < 4; ++e) uld(k, e);
      sfor<0, NP>([&](auto K_) {
        constexpr int k = decltype(K_)::v;
        bf16x8 wf[4];
#pragma unroll
        for (int cb = 0; cb < 4; ++cb) {
          const unsigned a = (wb ^ (unsigned)(cb << 5)) + bbs + (unsigned)pk[k] * 4096u;
          wf[cb] = ldstr2(a, a + 2048);
        }
        wait_vm<4 * ((k + UD <= NP ? UD : NP - k) - 1)>();
#pragma unroll
        for (int qq = 0; qq < 2; ++qq)
#pragma unroll
          for (int t = 0; t < 2; ++t) pin(uf[k][qq][t]);
#pragma unroll
        for (int cb = 0; cb < 4; ++cb) {
#pragma unroll
          for (int qq = 0; qq < 2; ++qq)
#pragma unroll
            for (int t = 0; t < 2; ++t) ab[cb][t] = mfma32(wf[cb], asbf(uf[k][qq][t]), ab[cb][t]);
          SCHED();
          if constexpr (k + UD < NP) uld(k + UD, cb);
          SCHED();
        }
      });
    };
    if (np == 6) bpart(IC<6>{}); else bpart(IC<5>{});
    if (bid == 0 && w == 7) {
#pragma unroll
      for (int k = 0; k < 4; ++k) wait_flags(ws, F_Z1 + 64 * k + lane, tag);
      if (lane == 0) __hip_atomic_store((GAS u64*)(ws + WS_GEN), tag, __ATOMIC_RELAXED, __HIP_MEMORY_SCOPE_AGENT);
    }
    bar();
    constexpr unsigned RP = 272;
#pragma unroll
    for (int t = 0; t < 2; ++t) {
      const float g = __uint_as_float(gg[t]), g1 = 1.0f - g;
      const unsigned ro = L0 + (unsigned)(kq * 64 + 32 * t2 + 2 * c16 + t) * RP + (unsigned)q4 * 16u;
#pragma unroll
      for (int cb = 0; cb < 4; ++cb) {
        f32x4 v;
#pragma unroll
        for (int r = 0; r < 4; ++r) v[r] = g * aa[cb][t][r] + g1 * ab[cb][t][r];
        lds_st128(ro + (unsigned)cb * 64u, v);
      }
    }
    bar();
    {
      const int tok = tid >> 3, chn = tid & 7;
      f32x4 s0 = f32x4{0, 0, 0, 0}, s1 = f32x4{0, 0, 0, 0};
#pragma unroll
      for (int k = 0; k < 4; ++k) {
        const unsigned ro = L0 + (unsigned)(k * 64 + tok) * RP + (unsigned)chn * 32u;
        s0 += lds128f(ro); s1 += lds128f(ro + 16);
      }
      u32x2 a = pk4(s0), b = pk4(s1);
#define OUT_AUX 16
      const srd_t sOUT = __builtin_amdgcn_make_buffer_rsrc((void*)out_, 0, 2048 * 512 * 2, 0x00020000);
      __builtin_amdgcn_raw_buffer_store_b128(u32x4{a[0], a[1], b[0], b[1]}, sOUT, (unsigned)(((64 * i3 + tok) * 512 + 64 * j3 + 8 * chn) * 2), 0, OUT_AUX);
    }
    asm volatile("mk_code_end:" ::: "memory");
  }
}

extern "C" void kernel_launch(void* const* d_in, const int* in_sizes, int n_in, void* d_out, int out_size, void* d_ws, size_t ws_size,
                              hipStream_t stream) {
  const __hip_bfloat16* x = (const __hip_bfloat16*)d_in[0];
  const __hip_bfloat16* wqkv = (const __hip_bfloat16*)d_in[1];
  const __hip_bfloat16* wo = (const __hip_bfloat16*)d_in[2];
  const __hip_bfloat16* win = (const __hip_bfloat16*)d_in[4];
  const __hip_bfloat16* wsp = (const __hip_bfloat16*)d_in[5];
  const __hip_bfloat16* bsp = (const __hip_bfloat16*)d_in[6];
  const __hip_bfloat16* wout = (const __hip_bfloat16*)d_in[7];
  const __hip_bfloat16* gw = (const __hip_bfloat16*)d_in[8];
  (void)hipFuncSetAttribute((const void*)mk_kernel, hipFuncAttributeMaxDynamicSharedMemorySize, LDS_BYTES);
  char* wsb = (char*)d_ws;
  mk_kernel<<<256, 512, LDS_BYTES, stream>>>(x, wqkv, wo, win, wsp, bsp, wout, gw, (__hip_bfloat16*)d_out, wsb);
}
```
